```python
import torch

def _softmax(x, axis=-1):
    return torch.softmax(x, dim=axis)
def _gelu(x):
    return torch.nn.functional.gelu(x, approximate="tanh")
def _silu(x):
    return torch.nn.functional.silu(x)
def _sigmoid(x):
    return torch.sigmoid(x)
def _layernorm(x, axis=-1, eps=1e-5):
    m = x.mean(dim=axis, keepdim=True)
    v = x.var(dim=axis, keepdim=True, unbiased=False)
    return (x - m) / torch.sqrt(v + eps)
def _scatter_add(tgt, idx, src, axis=0):
    return tgt.clone().index_add_(axis, idx, src)
def _segment_sum(src, seg, n):
    out = torch.zeros((n,) + src.shape[1:], dtype=src.dtype)
    return out.index_add_(0, seg, src)
def _pad(x, pw):
    flat = tuple(v for pair in reversed(pw) for v in pair)
    return torch.nn.functional.pad(x, flat)

kernel_name = 'gated_mix__sliding_local_dilated__dense_activation_mish__prompt'

def setup_inputs():

    _w = (lambda a: (lambda t: t.to(torch.bfloat16) if t.is_floating_point() else t)(torch.as_tensor(a)))
    def _A_setup():
        B, S, D = 1, 2048, 640
        return {
            "x": _w((lambda *s: torch.randn(*s, dtype=torch.bfloat16) * 0.05)(B, S, D)),
            "wqkv": _w((lambda *s: torch.randn(*s, dtype=torch.bfloat16) * 0.8)(D, 3 * D)),
            "wo": _w((lambda *s: torch.randn(*s, dtype=torch.bfloat16) * 0.05)(D, D)),
        }
    _A_in = _A_setup()

    def _B_setup():
        B, S, D, F = 1, 2048, 640, 1728
        return {
            "x": _w((lambda *s: torch.randn(*s, dtype=torch.bfloat16) * 0.05)(B, S, D)),
            "w1": _w((lambda *s: torch.randn(*s, dtype=torch.bfloat16) * 12.8)(D, F) * (D ** -0.5)),
            "b1": _w((lambda *s: torch.randn(*s, dtype=torch.bfloat16) * 0.8)(F)),
            "w2": _w((lambda *s: torch.randn(*s, dtype=torch.bfloat16) * 0.8)(F, D) * (F ** -0.5)),
        }
    _B_in = _B_setup()

    return {
        "A_x": _A_in["x"],
        "A_wqkv": _A_in["wqkv"],
        "A_wo": _A_in["wo"],
        "B_x": _B_in["x"],
        "B_w1": _B_in["w1"],
        "B_b1": _B_in["b1"],
        "B_w2": _B_in["w2"],
        "gate_w": _w((lambda *s: torch.randn(*s, dtype=torch.bfloat16) * 0.05)(640) * 16.0),
    }

def reference(A_x, A_wqkv, A_wo, B_x, B_w1, B_b1, B_w2, gate_w):
    def _A_ref(x, wqkv, wo):
        B, S, D = x.shape
        H = 32; Dh = D // H
        W, dil = 8, 2
        qkv = (lambda x, *d: x.permute(*d))((x @ wqkv).reshape(B, S, 3, H, Dh), 2, 0, 3, 1, 4)
        q, k, v = qkv[0], qkv[1], qkv[2]
        scale = Dh ** -0.5
        pos = torch.arange(S)
        off = torch.arange(W) * dil
        idx = pos[:, None] - off[None, :]
        mask = idx >= 0
        idx_c = torch.where(mask, idx, 0).reshape(-1)
        kg = k[:, :, idx_c, :].reshape(B, H, S, W, Dh)
        vg = v[:, :, idx_c, :].reshape(B, H, S, W, Dh)
        logits = torch.einsum("bhsd,bhswd->bhsw", q, kg) * scale
        logits = torch.where(mask[None, None, :, :], logits, -1e9)
        attn = _softmax(logits, axis=-1)
        o = torch.einsum("bhsw,bhswd->bhsd", attn, vg)
        return (lambda x, *d: x.permute(*d))(o, 0, 2, 1, 3).reshape(B, S, D) @ wo

    def _B_ref(x, w1, b1, w2):
        h = torch.einsum("bsd,df->bsf", x, w1) + b1
        sp = torch.log1p(torch.exp(-torch.abs(h))) + torch.where(h > 0, h, torch.zeros_like(h))
        act = h * torch.tanh(sp)
        return torch.einsum("bsf,fd->bsd", act, w2)

    x = A_x
    g = _sigmoid((x * gate_w).sum(-1, keepdims=True))
    a = _A_ref(x, A_wqkv, A_wo)
    b = _B_ref(x, B_w1, B_b1, B_w2)
    return g * a + (1 - g) * b
```

```cpp
#include <hip/hip_runtime.h>
#include <hip/hip_bf16.h>
#include <stdint.h>

namespace {

typedef __attribute__((ext_vector_type(4))) short s16x4;
typedef __attribute__((ext_vector_type(8))) __bf16 bf16x8;
typedef __attribute__((ext_vector_type(2))) __bf16 bf16x2;
typedef __attribute__((ext_vector_type(4))) float f32x4;
typedef __attribute__((ext_vector_type(2))) float f32x2;
typedef __attribute__((ext_vector_type(4))) unsigned u32x4;
typedef __attribute__((ext_vector_type(2))) unsigned u32x2;
#define LAS __attribute__((address_space(3)))

constexpr int S_TOK = 2048, D = 640, F = 1728;
constexpr int KS2 = 74;
constexpr size_t A2_BYTES = (size_t)128 * KS2 * 1024;
constexpr size_t B2_BYTES = (size_t)40 * KS2 * 1024;
constexpr size_t A2_OFF = 0, B2_OFF = A2_OFF + A2_BYTES, G_OFF = B2_OFF + B2_BYTES;

constexpr int X_BYTES = 144 * 128;
constexpr int W_ROW = 480;
constexpr int W_BYTES = 64 * W_ROW;
constexpr int STAGE = X_BYTES + W_BYTES;
constexpr int NSTAGE = 3;
constexpr int EX_OFF = NSTAGE * STAGE;
constexpr int EX_Q = EX_OFF, EX_K = EX_Q + 8 * 512, EX_V = EX_K + 9 * 512;
constexpr int K1_LDS = EX_V + 9 * 512;
constexpr int NT1 = 10;

__device__ __forceinline__ unsigned pack2(float a, float b) {
  f32x2 v = {a, b};
  bf16x2 r = __builtin_convertvector(v, bf16x2);
  return __builtin_bit_cast(unsigned, r);
}
__device__ __forceinline__ float bf_lo(unsigned u) { return __builtin_bit_cast(float, u << 16); }
__device__ __forceinline__ float bf_hi(unsigned u) { return __builtin_bit_cast(float, u & 0xffff0000u); }
__device__ __forceinline__ f32x4 mfma16(u32x4 a, u32x4 b, f32x4 c) {
  return __builtin_amdgcn_mfma_f32_16x16x32_bf16(__builtin_bit_cast(bf16x8, a), __builtin_bit_cast(bf16x8, b), c, 0, 0, 0);
}
__device__ __forceinline__ u32x4 lds_read128(unsigned addr) { return *(const LAS u32x4*)(uintptr_t)addr; }
__device__ __forceinline__ u32x2 lds_read64(unsigned addr) { return *(const LAS u32x2*)(uintptr_t)addr; }
__device__ __forceinline__ u32x2 lds_tr(unsigned addr) {
  s16x4 v = __builtin_amdgcn_ds_read_tr16_b64_v4i16((LAS s16x4*)(uintptr_t)addr);
  return __builtin_bit_cast(u32x2, v);
}
__device__ __forceinline__ u32x4 lds_wfrag(unsigned addr) {
  u32x2 a = lds_tr(addr), b = lds_tr(addr + 8 * W_ROW);
  u32x4 r = {a.x, a.y, b.x, b.y};
  return r;
}
__device__ __forceinline__ void dma16(unsigned ldsaddr, unsigned voff, u32x4 rsrc, unsigned soff) {
  asm volatile("s_mov_b32 m0, %0\n\ts_nop 0\n\tbuffer_load_dwordx4 %1, %2, %3 offen lds"
               :: "s"(ldsaddr), "v"(voff), "s"(rsrc), "s"(soff) : "memory");
}
#define WAIT_V(n) asm volatile("s_waitcnt vmcnt(" #n ")" ::: "memory")
#define WAIT_L0() asm volatile("s_waitcnt lgkmcnt(0)" ::: "memory")
#define BAR() __builtin_amdgcn_s_barrier()
#define SCHED() __builtin_amdgcn_sched_barrier(0)

__device__ __forceinline__ u32x4 make_rsrc(const void* p, unsigned bytes) {
  uint64_t a = (uint64_t)p;
  u32x4 r;
  r.x = __builtin_amdgcn_readfirstlane((unsigned)a);
  r.y = __builtin_amdgcn_readfirstlane((unsigned)(a >> 32) & 0xffffu);
  r.z = bytes;
  r.w = 0x00020000u;
  return r;
}

struct Stager {
  unsigned voff[12];
  u32x4 rx, rw, r4;
  unsigned l4;
  unsigned lx, lw;
  unsigned wstep, step4;
};

template <int P>
__device__ __forceinline__ void stage_piece(const Stager& st, unsigned sb, int t) {
  if constexpr (P < 4) dma16(sb + st.lx + P * 4096, st.voff[P], st.rx, (unsigned)t * 128u);
  else if constexpr (P == 4) dma16(sb + st.l4, st.voff[4], st.r4, (unsigned)t * st.step4);
  else dma16(sb + st.lw + (P - 5) * 4096, st.voff[P], st.rw, (unsigned)t * st.wstep);
}
__device__ __forceinline__ void stage_all(const Stager& st, unsigned sb, int t) {
  stage_piece<0>(st, sb, t); stage_piece<5>(st, sb, t); stage_piece<6>(st, sb, t);
  stage_piece<1>(st, sb, t); stage_piece<7>(st, sb, t); stage_piece<8>(st, sb, t);
  stage_piece<2>(st, sb, t); stage_piece<9>(st, sb, t); stage_piece<10>(st, sb, t);
  stage_piece<3>(st, sb, t); stage_piece<11>(st, sb, t); stage_piece<4>(st, sb, t);
}

__device__ __forceinline__ int swap23(int k) { return (k & ~12) | ((k & 4) << 1) | ((k & 8) >> 1); }

__device__ __forceinline__ unsigned x_src(int xi, int lane, int m0) {
  int row = xi * 8 + (lane >> 3);
  int c = (lane & 7) ^ ((row >> 1) & 7);
  int tok = row < 128 ? m0 + row : (m0 == 0 ? row - 128 : m0 - 144 + row);
  return (unsigned)(tok * (D * 2) + c * 16);
}
__device__ __forceinline__ unsigned w_src(int wi, int lane, bool attn, int ncol0) {
  int ci = wi * 64 + lane;
  int pos = ci / 30, ch = ci - pos * 30;
  int k = swap23(pos);
  if (attn) {
    int sec = ch / 10, cc = ch - sec * 10;
    return (unsigned)(k * (3 * D * 2) + (sec * D + ncol0) * 2 + cc * 16);
  }
  if (ch > 27) ch = 27;
  return (unsigned)(k * (F * 2) + ncol0 * 2 + ch * 16);
}

template <int W>
struct AttnFrags { u32x4 x[9]; u32x4 w[4]; };

template <int W, int SL>
__device__ __forceinline__ void attn_reads(AttnFrags<W>& f, unsigned xa, unsigned wa, unsigned we) {
  if constexpr (SL == 0) { f.w[1] = lds_wfrag(wa + 160); f.x[8] = lds_read128(xa + 8 * 2048); }
  if constexpr (SL == 1) { f.w[2] = lds_wfrag(wa + 320); f.x[0] = lds_read128(xa); }
  if constexpr (SL == 2) { f.w[0] = lds_wfrag(wa); f.x[1] = lds_read128(xa + 2048); }
  if constexpr (SL == 3) { if constexpr (W < 3) f.w[3] = lds_wfrag(we); f.x[2] = lds_read128(xa + 2 * 2048); }
  if constexpr (SL == 4) { f.x[3] = lds_read128(xa + 3 * 2048); f.x[4] = lds_read128(xa + 4 * 2048); f.x[5] = lds_read128(xa + 5 * 2048); }
  if constexpr (SL == 5) { f.x[6] = lds_read128(xa + 6 * 2048); f.x[7] = lds_read128(xa + 7 * 2048); }
}

struct AttnAcc { f32x4 q[8], k[9], v[9], e[9]; };

template <int W, int RB>
__device__ __forceinline__ void attn_mma_rb(AttnAcc& a, const AttnFrags<W>& f) {
  if constexpr (RB < 8) a.q[RB] = mfma16(f.w[0], f.x[RB], a.q[RB]);
  a.k[RB] = mfma16(f.w[1], f.x[RB], a.k[RB]);
  a.v[RB] = mfma16(f.x[RB], f.w[2], a.v[RB]);
  if constexpr (W == 0 && RB < 8) a.e[RB] = mfma16(f.w[3], f.x[RB], a.e[RB]);
  if constexpr (W == 1) a.e[RB] = mfma16(f.w[3], f.x[RB], a.e[RB]);
  if constexpr (W == 2) a.e[RB] = mfma16(f.x[RB], f.w[3], a.e[RB]);
}
template <int W, int SL>
__device__ __forceinline__ void attn_mma(AttnAcc& a, const AttnFrags<W>& f) {
  if constexpr (SL == 0) { attn_mma_rb<W, 8>(a, f); attn_mma_rb<W, 0>(a, f); }
  if constexpr (SL == 1) { attn_mma_rb<W, 1>(a, f); }
  if constexpr (SL == 2) { attn_mma_rb<W, 2>(a, f); attn_mma_rb<W, 3>(a, f); }
  if constexpr (SL == 3) { attn_mma_rb<W, 4>(a, f); }
  if constexpr (SL == 4) { attn_mma_rb<W, 5>(a, f); attn_mma_rb<W, 6>(a, f); }
  if constexpr (SL == 5) { attn_mma_rb<W, 7>(a, f); }
}

struct MlpFrags { u32x4 x[4]; u32x4 w[7]; };
struct MlpAcc { f32x4 c[4][7]; };
template <int SL>
__device__ __forceinline__ void mlp_reads(MlpFrags& f, unsigned xa, unsigned wa) {
  if constexpr (SL == 0) { f.w[0] = lds_wfrag(wa); f.x[0] = lds_read128(xa); }
  if constexpr (SL == 1) { f.x[1] = lds_read128(xa + 2048); f.x[2] = lds_read128(xa + 2 * 2048); f.x[3] = lds_read128(xa + 3 * 2048); }
  if constexpr (SL == 2) { f.w[1] = lds_wfrag(wa + 32); f.w[2] = lds_wfrag(wa + 64); }
  if constexpr (SL == 3) { f.w[3] = lds_wfrag(wa + 96); }
  if constexpr (SL == 4) { f.w[4] = lds_wfrag(wa + 128); f.w[5] = lds_wfrag(wa + 160); }
  if constexpr (SL == 5) { f.w[6] = lds_wfrag(wa + 192); }
}
template <int NB>
__device__ __forceinline__ void mlp_mma_nb(MlpAcc& a, const MlpFrags& f) {
#pragma unroll
  for (int r = 0; r < 4; ++r) a.c[r][NB] = mfma16(f.w[NB], f.x[r], a.c[r][NB]);
}
template <int SL>
__device__ __forceinline__ void mlp_mma(MlpAcc& a, const MlpFrags& f) {
  if constexpr (SL == 0) { mlp_mma_nb<0>(a, f); }
  if constexpr (SL == 1) { mlp_mma_nb<1>(a, f); }
  if constexpr (SL == 2) { mlp_mma_nb<2>(a, f); }
  if constexpr (SL == 3) { mlp_mma_nb<3>(a, f); }
  if constexpr (SL == 4) { mlp_mma_nb<4>(a, f); mlp_mma_nb<5>(a, f); }
  if constexpr (SL == 5) { mlp_mma_nb<6>(a, f); }
}

template <int SL>
__device__ __forceinline__ void stage_slice(const Stager& st, unsigned sb, int t) {
  if constexpr (SL == 0) { stage_piece<0>(st, sb, t); stage_piece<5>(st, sb, t); }
  if constexpr (SL == 1) { stage_piece<6>(st, sb, t); stage_piece<1>(st, sb, t); }
  if constexpr (SL == 2) { stage_piece<7>(st, sb, t); stage_piece<8>(st, sb, t); }
  if constexpr (SL == 3) { stage_piece<2>(st, sb, t); stage_piece<9>(st, sb, t); }
  if constexpr (SL == 4) { stage_piece<10>(st, sb, t); stage_piece<3>(st, sb, t); }
  if constexpr (SL == 5) { stage_piece<11>(st, sb, t); stage_piece<4>(st, sb, t); }
}

template <class OPS>
__device__ __forceinline__ void k_loop(OPS& o, const Stager& st) {
  typename OPS::Frags f0, f1;
  WAIT_V(24);
  BAR();
  o.template reads<0>(f0, 0, 0); o.template reads<1>(f0, 0, 0); o.template reads<2>(f0, 0, 0);
  o.template reads<3>(f0, 0, 0); o.template reads<4>(f0, 0, 0); o.template reads<5>(f0, 0, 0);
  unsigned sb = 0;
#pragma unroll 1
  for (int t = 0; t < NT1; ++t) {
    unsigned sn = sb + STAGE; if (sn == NSTAGE * STAGE) sn = 0;
    SCHED();
#define HALF_A(SL) o.template reads<SL>(f1, sb, 1); o.template mma<SL>(f0); SCHED();
    HALF_A(0) HALF_A(1) HALF_A(2) HALF_A(3) HALF_A(4) HALF_A(5)
#undef HALF_A
    WAIT_L0();
    if (t < NT1 - 2) { WAIT_V(12); } else { WAIT_V(0); }
    BAR();
    SCHED();
    if (t < NT1 - 3) {
#define HALF_B(SL) o.template reads<SL>(f0, sn, 0); stage_slice<SL>(st, sb, t + 3); o.template mma<SL>(f1); SCHED();
      HALF_B(0) HALF_B(1) HALF_B(2) HALF_B(3) HALF_B(4) HALF_B(5)
#undef HALF_B
    } else {
#define HALF_B(SL) o.template reads<SL>(f0, sn, 0); o.template mma<SL>(f1); SCHED();
      HALF_B(0) HALF_B(1) HALF_B(2) HALF_B(3) HALF_B(4) HALF_B(5)
#undef HALF_B
    }
    sb = sn;
  }
}

template <int W>
struct AttnOps {
  typedef AttnFrags<W> Frags;
  AttnAcc a;
  unsigned xa, wa, we;
  template <int SL> __device__ __forceinline__ void reads(Frags& f, unsigned sb, int kk) {
    attn_reads<W, SL>(f, (xa ^ (kk * 64)) + sb, wa + sb + kk * 32 * W_ROW, we + sb + kk * 32 * W_ROW);
  }
  template <int SL> __device__ __forceinline__ void mma(const Frags& f) { attn_mma<W, SL>(a, f); }
};
struct MlpOps {
  typedef MlpFrags Frags;
  MlpAcc a;
  unsigned xa, wa;
  template <int SL> __device__ __forceinline__ void reads(Frags& f, unsigned sb, int kk) {
    mlp_reads<SL>(f, (xa ^ (kk * 64)) + sb, wa + sb + kk * 32 * W_ROW);
  }
  template <int SL> __device__ __forceinline__ void mma(const Frags& f) { mlp_mma<SL>(a, f); }
};

__device__ __forceinline__ float xmax4(float x) {
  unsigned u = __builtin_bit_cast(unsigned, x);
  auto r = __builtin_amdgcn_permlane32_swap(u, u, false, false);
  unsigned r0 = r[0], r1 = r[1];
  x = fmaxf(__builtin_bit_cast(float, r0), __builtin_bit_cast(float, r1));
  u = __builtin_bit_cast(unsigned, x);
  auto s = __builtin_amdgcn_permlane16_swap(u, u, false, false);
  unsigned s0 = s[0], s1 = s[1];
  return fmaxf(__builtin_bit_cast(float, s0), __builtin_bit_cast(float, s1));
}
__device__ __forceinline__ float xsum4(float x) {
  unsigned u = __builtin_bit_cast(unsigned, x);
  auto r = __builtin_amdgcn_permlane32_swap(u, u, false, false);
  unsigned r0 = r[0], r1 = r[1];
  x = __builtin_bit_cast(float, r0) + __builtin_bit_cast(float, r1);
  u = __builtin_bit_cast(unsigned, x);
  auto s = __builtin_amdgcn_permlane16_swap(u, u, false, false);
  unsigned s0 = s[0], s1 = s[1];
  return __builtin_bit_cast(float, s0) + __builtin_bit_cast(float, s1);
}

template <int W>
__device__ __forceinline__ void attn_role(const Stager& st, int lane, int mt, int hg, unsigned char* __restrict__ ws) {
  const int c = lane & 15, g = lane >> 4, q4 = (lane >> 2) & 3, p4 = lane & 3;
  AttnOps<W> o;
#pragma unroll
  for (int i = 0; i < 9; ++i) {
    if (i < 8) o.a.q[i] = f32x4{0.f, 0.f, 0.f, 0.f};
    o.a.k[i] = f32x4{0.f, 0.f, 0.f, 0.f}; o.a.v[i] = f32x4{0.f, 0.f, 0.f, 0.f}; o.a.e[i] = f32x4{0.f, 0.f, 0.f, 0.f};
  }
  o.xa = c * 128 + ((g ^ ((c >> 1) & 7)) * 16);
  const unsigned wrow = X_BYTES + ((g >> 1) * 16 + (g & 1) * 4 + q4) * W_ROW;
  o.wa = wrow + 40 * W + 8 * p4;
  o.we = wrow + 160 * (W < 3 ? W : 0) + 40 * p4 + 32;
  k_loop(o, st);

  if constexpr (W < 3) {
    constexpr int base = W == 0 ? EX_Q : (W == 1 ? EX_K : EX_V);
#pragma unroll
    for (int i = 0; i < (W == 0 ? 8 : 9); ++i) {
      u32x2 v = {pack2(o.a.e[i][0], o.a.e[i][1]), pack2(o.a.e[i][2], o.a.e[i][3])};
      *(LAS u32x2*)(uintptr_t)(base + i * 512 + lane * 8) = v;
    }
  }
  WAIT_L0();
  BAR();

  f32x4 mprev, mcur;
#pragma unroll
  for (int r = 0; r < 4; ++r) {
    int dcur = c - 4 * g - r, dprev = dcur + 16;
    mcur[r] = (dcur >= 0 && (dcur & 1) == 0) ? 0.f : -1e30f;
    mprev[r] = (dprev <= 14 && (dprev & 1) == 0) ? 0.f : -1e30f;
  }
  const float c2 = 0.22360679774997896f * 1.4426950408889634f;
  const float even = (c & 1) ? 0.f : 1.f, odd = 1.f - even;
  const unsigned exa = W * 128 + c * 8 + g * 2;

  u32x4 kf[9]; u32x2 vp[9], vx[9];
#pragma unroll
  for (int i = 0; i < 9; ++i) {
    kf[i].x = pack2(o.a.k[i][0], o.a.k[i][1]); kf[i].y = pack2(o.a.k[i][2], o.a.k[i][3]);
    kf[i].z = *(const LAS unsigned short*)(uintptr_t)(EX_K + i * 512 + exa); kf[i].w = 0u;
    vp[i].x = pack2(o.a.v[i][0], o.a.v[i][1]); vp[i].y = pack2(o.a.v[i][2], o.a.v[i][3]);
    vx[i] = lds_read64(EX_V + i * 512 + lane * 8);
  }
  const int H = hg * 4 + W;
  unsigned char* a2 = ws + A2_OFF + (size_t)(mt * 8) * KS2 * 1024 + lane * 16;
#pragma unroll
  for (int i = 0; i < 8; ++i) {
    const int ip = i == 0 ? 8 : i - 1;
    u32x4 qf;
    qf.x = pack2(o.a.q[i][0], o.a.q[i][1]); qf.y = pack2(o.a.q[i][2], o.a.q[i][3]);
    qf.z = *(const LAS unsigned short*)(uintptr_t)(EX_Q + i * 512 + exa); qf.w = 0u;
    f32x4 mp = mprev;
    if (i == 0 && mt == 0) mp = f32x4{-1e30f, -1e30f, -1e30f, -1e30f};
    f32x4 sp = mfma16(kf[ip], qf, mp);
    f32x4 sc = mfma16(kf[i], qf, mcur);
    float t0 = fmaxf(sp[0], sp[1]), t1 = fmaxf(sp[2], sp[3]), t2 = fmaxf(sc[0], sc[1]), t3 = fmaxf(sc[2], sc[3]);
    float m = xmax4(fmaxf(fmaxf(t0, t1), fmaxf(t2, t3)));
    float mc = m * c2;
    float p0 = __builtin_amdgcn_exp2f(fmaf(t0, c2, -mc)), p1 = __builtin_amdgcn_exp2f(fmaf(t1, c2, -mc));
    float p2 = __builtin_amdgcn_exp2f(fmaf(t2, c2, -mc)), p3 = __builtin_amdgcn_exp2f(fmaf(t3, c2, -mc));
    float rinv = __builtin_amdgcn_rcpf(xsum4((p0 + p1) + (p2 + p3)));
    float re = rinv * even, ro = rinv * odd;
    u32x4 pf = {pack2(p0 * re, p0 * ro), pack2(p1 * re, p1 * ro), pack2(p2 * re, p2 * ro), pack2(p3 * re, p3 * ro)};
    u32x4 vf = {vp[ip].x, vp[ip].y, vp[i].x, vp[i].y};
    u32x4 xf = {vx[ip].x, vx[ip].y, vx[i].x, vx[i].y};
    f32x4 z = {0.f, 0.f, 0.f, 0.f};
    f32x4 om = mfma16(vf, pf, z);
    f32x4 ox = mfma16(xf, pf, z);
    u32x2 s0 = {pack2(om[0], om[1]), pack2(om[2], om[3])};
    *(u32x2*)(a2 + ((size_t)i * KS2 + (H >> 1)) * 1024 + (H & 1) * 8) = s0;
    if (g == W) {
      u32x2 s1 = {pack2(ox[0], ox[1]), pack2(ox[2], ox[3])};
      *(u32x2*)(a2 + ((size_t)i * KS2 + 16 + (hg >> 1)) * 1024 + (hg & 1) * 8) = s1;
    }
  }
}

__device__ __forceinline__ float mish(float h) {
  float u = __builtin_amdgcn_exp2f(fminf(h * 1.4426950408889634f, 60.f));
  float n = u * (u + 2.f);
  return h * n * __builtin_amdgcn_rcpf(n + 2.f);
}

__device__ __forceinline__ void mlp_role(const Stager& st, int lane, int wave, int mt, int nb0, u32x2 (&bias)[7],
                                         unsigned char* __restrict__ ws) {
  const int c = lane & 15, g = lane >> 4, q4 = (lane >> 2) & 3, p4 = lane & 3;
  const int wm = wave >> 1, wn = wave & 1;
  MlpOps o;
  asm volatile("s_waitcnt vmcnt(36)" : "+v"(bias[0]), "+v"(bias[1]), "+v"(bias[2]), "+v"(bias[3]), "+v"(bias[4]), "+v"(bias[5]), "+v"(bias[6]));
#pragma unroll
  for (int n = 0; n < 7; ++n) {
    f32x4 b = {bf_lo(bias[n].x), bf_hi(bias[n].x), bf_lo(bias[n].y), bf_hi(bias[n].y)};
#pragma unroll
    for (int r = 0; r < 4; ++r) o.a.c[r][n] = b;
  }
  o.xa = wm * 4 * 2048 + c * 128 + ((g ^ ((c >> 1) & 7)) * 16);
  o.wa = X_BYTES + ((g >> 1) * 16 + (g & 1) * 4 + q4) * W_ROW + wn * 7 * 32 + 8 * p4;
  k_loop(o, st);

  unsigned char* a2 = ws + A2_OFF + ((size_t)(mt * 8 + wm * 4) * KS2 + 20 + (nb0 >> 1)) * 1024 + lane * 16;
#pragma unroll
  for (int r = 0; r < 4; ++r) {
    u32x2 pk[7];
#pragma unroll
    for (int n = 0; n < 7; ++n) {
      f32x4 h = o.a.c[r][n];
      pk[n].x = pack2(mish(h[0]), mish(h[1]));
      pk[n].y = pack2(mish(h[2]), mish(h[3]));
    }
    unsigned char* p = a2 + (size_t)r * KS2 * 1024;
    if (wn == 0) {
#pragma unroll
      for (int j = 0; j < 3; ++j) *(u32x4*)(p + j * 1024) = u32x4{pk[2 * j].x, pk[2 * j].y, pk[2 * j + 1].x, pk[2 * j + 1].y};
      *(u32x2*)(p + 3 * 1024) = pk[6];
    } else {
      *(u32x2*)(p + 3 * 1024 + 8) = pk[0];
#pragma unroll
      for (int j = 0; j < 3; ++j) *(u32x4*)(p + (4 + j) * 1024) = u32x4{pk[2 * j + 1].x, pk[2 * j + 1].y, pk[2 * j + 2].x, pk[2 * j + 2].y};
    }
  }
}

__device__ __forceinline__ const unsigned short* w2cat_row(const unsigned short* wo, const unsigned short* w2, int beta, int rho) {
  if (beta < 32) return wo + (20 * beta + rho) * D;
  if (beta < 40) return wo + (20 * (4 * (beta - 32) + (rho >> 2)) + 16 + (rho & 3)) * D;
  return w2 + (16 * (beta - 40) + rho) * D;
}

__global__ __launch_bounds__(256, 1) void k1_kernel(const unsigned short* __restrict__ x, const unsigned short* __restrict__ wqkv,
                                                    const unsigned short* __restrict__ wo, const unsigned short* __restrict__ w1,
                                                    const unsigned short* __restrict__ b1, const unsigned short* __restrict__ w2,
                                                    const unsigned short* __restrict__ gate_w, unsigned char* __restrict__ ws) {
  extern __shared__ __attribute__((aligned(16))) unsigned char smem[];
  const int lane = threadIdx.x & 63;
  const int wave = __builtin_amdgcn_readfirstlane(threadIdx.x >> 6);
  const int b = blockIdx.x, xcd = b & 7, idx = b >> 3;
  const int mt = (xcd & 1) * 8 + (idx & 7);
  const int nsel = idx >> 3;
  const int nt = (xcd >> 1) * 2 + (nsel & 1);
  const bool attn = nsel < 2;
  const int m0 = mt * 128;
  const int nb0 = nt * 14 < 94 ? nt * 14 : 94;

  u32x2 bias[7];
  if (!attn) {
    const unsigned short* bp = b1 + (nb0 + (wave & 1) * 7) * 16 + (lane >> 4) * 4;
#pragma unroll
    for (int n = 0; n < 7; ++n) asm volatile("global_load_dwordx2 %0, %1, off offset:%2" : "=v"(bias[n]) : "v"(bp), "i"(n * 32) : "memory");
  }

  Stager st;
  st.rx = make_rsrc(x, S_TOK * D * 2);
  st.rw = attn ? make_rsrc(wqkv, D * 3 * D * 2) : make_rsrc(w1, D * F * 2);
  st.wstep = attn ? 64u * 3 * D * 2 : 64u * F * 2;
  const int ncol0 = attn ? nt * 80 : nb0 * 16;
#pragma unroll
  for (int p = 0; p < 4; ++p) st.voff[p] = x_src(wave + 4 * p, lane, m0);
#pragma unroll
  for (int j = 0; j < 7; ++j) st.voff[5 + j] = w_src(wave + 4 * j, lane, attn, ncol0);
  st.lx = wave * 1024; st.lw = X_BYTES + wave * 1024;
  if (wave < 2) { st.voff[4] = x_src(16 + wave, lane, m0); st.r4 = st.rx; st.step4 = 128u; st.l4 = (16 + wave) * 1024; }
  else { st.voff[4] = w_src(26 + wave, lane, attn, ncol0); st.r4 = st.rw; st.step4 = st.wstep; st.l4 = X_BYTES + (26 + wave) * 1024; }
  stage_all(st, 0, 0);
  stage_all(st, STAGE, 1);
  stage_all(st, 2 * STAGE, 2);

  if (attn) {
    if (wave == 0) attn_role<0>(st, lane, mt, nt, ws);
    else if (wave == 1) attn_role<1>(st, lane, mt, nt, ws);
    else if (wave == 2) attn_role<2>(st, lane, mt, nt, ws);
    else attn_role<3>(st, lane, mt, nt, ws);
  } else {
    mlp_role(st, lane, wave, mt, nb0, bias, ws);
  }

  {
    const int gw = b * 4 + wave;
    const int c = lane & 15, g = lane >> 4;
#pragma unroll
    for (int i = 0; i < 3; ++i) {
      int f = gw * 3 + i;
      if (f < 40 * KS2) {
        int ks = f / 40, nb = f - ks * 40;
        unsigned e[8];
#pragma unroll
        for (int j = 0; j < 8; ++j) e[j] = w2cat_row(wo, w2, 2 * ks + (j >> 2), 4 * g + (j & 3))[nb * 16 + c];
        u32x4 v = {e[0] | (e[1] << 16), e[2] | (e[3] << 16), e[4] | (e[5] << 16), e[6] | (e[7] << 16)};
        *(u32x4*)(ws + B2_OFF + ((size_t)nb * KS2 + ks) * 1024 + lane * 16) = v;
      }
    }
    const int ntile16 = (attn ? 0 : 8) + nt;
#pragma unroll
    for (int i = 0; i < 2; ++i) {
      int tok = m0 + ntile16 * 8 + wave * 2 + i;
      const unsigned* xr = (const unsigned*)(x + (size_t)tok * D);
      const unsigned* gw2 = (const unsigned*)gate_w;
      float s = 0.f;
#pragma unroll
      for (int j = 0; j < 5; ++j) {
        unsigned a = xr[lane + 64 * j], w = gw2[lane + 64 * j];
        s = fmaf(bf_lo(a), bf_lo(w), s);
        s = fmaf(bf_hi(a), bf_hi(w), s);
      }
#pragma unroll
      for (int o = 32; o >= 1; o >>= 1) s += __shfl_xor(s, o, 64);
      if (lane == 0) ((float*)(ws + G_OFF))[tok] = 1.f / (1.f + __expf(-s));
    }
  }
}

constexpr int K2_DEPTH = 6;
template <int NSTEP>
__device__ __forceinline__ void k2_stream(f32x4 (&acc)[4][5], const u32x4* __restrict__ ap, const u32x4* __restrict__ bp) {
  u32x4 fa[K2_DEPTH][4], fb[K2_DEPTH][5];
#pragma unroll
  for (int s = 0; s < K2_DEPTH; ++s) {
#pragma unroll
    for (int i = 0; i < 4; ++i) fa[s][i] = ap[(i * KS2 + s) * 64];
#pragma unroll
    for (int j = 0; j < 5; ++j) fb[s][j] = bp[(j * KS2 + s) * 64];
  }
  SCHED();
#pragma unroll
  for (int s = 0; s < NSTEP; ++s) {
    const int r = s % K2_DEPTH;
#pragma unroll
    for (int i = 0; i < 4; ++i)
#pragma unroll
      for (int j = 0; j < 5; ++j) acc[i][j] = mfma16(fb[r][j], fa[r][i], acc[i][j]);
    SCHED();
    if (s + K2_DEPTH < NSTEP) {
#pragma unroll
      for (int i = 0; i < 4; ++i) fa[r][i] = ap[(i * KS2 + s + K2_DEPTH) * 64];
#pragma unroll
      for (int j = 0; j < 5; ++j) fb[r][j] = bp[(j * KS2 + s + K2_DEPTH) * 64];
    }
    SCHED();
  }
}

__global__ __launch_bounds__(256, 1) void k2_kernel(const unsigned char* __restrict__ ws, unsigned short* __restrict__ out) {
  __shared__ __attribute__((aligned(16))) f32x4 red[4][20][64];
  const int lane = threadIdx.x & 63;
  const int wave = __builtin_amdgcn_readfirstlane(threadIdx.x >> 6);
  const int b = blockIdx.x, xcd = b & 7, idx = b >> 3;
  const int mt = (xcd & 3) * 8 + (idx & 7);
  const int nt = (xcd >> 2) * 4 + (idx >> 3);
  const int ks0 = wave == 0 ? 0 : 20 + (wave - 1) * 18;
  const u32x4* ap = (const u32x4*)(ws + A2_OFF) + ((size_t)(mt * 4) * KS2 + ks0) * 64 + lane;
  const u32x4* bp = (const u32x4*)(ws + B2_OFF) + ((size_t)(nt * 5) * KS2 + ks0) * 64 + lane;
  const int c = lane & 15, g = lane >> 4;
  const float gate = ((const float*)(ws + G_OFF))[mt * 64 + wave * 16 + c];
  f32x4 acc[4][5];
#pragma unroll
  for (int i = 0; i < 4; ++i)
#pragma unroll
    for (int j = 0; j < 5; ++j) acc[i][j] = f32x4{0.f, 0.f, 0.f, 0.f};
  if (wave == 0) k2_stream<20>(acc, ap, bp);
  else k2_stream<18>(acc, ap, bp);
#pragma unroll
  for (int i = 0; i < 4; ++i)
#pragma unroll
    for (int j = 0; j < 5; ++j) red[wave][i * 5 + j][lane] = acc[i][j];
  __syncthreads();
  unsigned short* op = out + (size_t)(mt * 64 + wave * 16 + c) * D + nt * 80 + g * 4;
#pragma unroll
  for (int j = 0; j < 5; ++j) {
    f32x4 a = red[0][wave * 5 + j][lane];
    f32x4 m = red[1][wave * 5 + j][lane] + red[2][wave * 5 + j][lane] + red[3][wave * 5 + j][lane];
    f32x4 r = a * gate + m * (1.f - gate);
    u32x2 s = {pack2(r[0], r[1]), pack2(r[2], r[3])};
    *(u32x2*)(op + j * 16) = s;
  }
}

}

extern "C" void kernel_launch(void* const* d_in, const int* in_sizes, int n_in, void* d_out, int out_size, void* d_ws,
                              size_t ws_size, hipStream_t stream) {
  const unsigned short* x = (const unsigned short*)d_in[0];
  const unsigned short* wqkv = (const unsigned short*)d_in[1];
  const unsigned short* wo = (const unsigned short*)d_in[2];
  const unsigned short* w1 = (const unsigned short*)d_in[4];
  const unsigned short* b1 = (const unsigned short*)d_in[5];
  const unsigned short* w2 = (const unsigned short*)d_in[6];
  const unsigned short* gate_w = (const unsigned short*)d_in[7];
  (void)hipFuncSetAttribute((const void*)k1_kernel, hipFuncAttributeMaxDynamicSharedMemorySize, K1_LDS);
  k1_kernel<<<256, 256, K1_LDS, stream>>>(x, wqkv, wo, w1, b1, w2, gate_w, (unsigned char*)d_ws);
  k2_kernel<<<256, 256, 0, stream>>>((const unsigned char*)d_ws, (unsigned short*)d_out);
}
```

```cpp
#include <hip/hip_runtime.h>
#include <hip/hip_bf16.h>
#include <stdint.h>

#define STAMP(ws, kern, i)
#define NO_PF2
#define PF_XCD

namespace {

typedef __attribute__((ext_vector_type(4))) short s16x4;
typedef __attribute__((ext_vector_type(8))) __bf16 bf16x8;
typedef __attribute__((ext_vector_type(2))) __bf16 bf16x2;
typedef __attribute__((ext_vector_type(4))) float f32x4;
typedef __attribute__((ext_vector_type(2))) float f32x2;
typedef __attribute__((ext_vector_type(4))) unsigned u32x4;
typedef __attribute__((ext_vector_type(2))) unsigned u32x2;
#define LAS __attribute__((address_space(3)))

constexpr int S_TOK = 2048, D = 640, F = 1728;
constexpr int KS2 = 74;
constexpr size_t A2_BYTES = (size_t)128 * KS2 * 1024;
constexpr size_t B2_BYTES = (size_t)40 * KS2 * 1024;
constexpr size_t A2_OFF = 0, B2_OFF = A2_OFF + A2_BYTES, G_OFF = B2_OFF + B2_BYTES;

constexpr int X_BYTES = 144 * 128;
constexpr int W_ROW = 480;
constexpr int W_BYTES = 64 * W_ROW;
constexpr int STAGE = X_BYTES + W_BYTES;
constexpr int NSTAGE = 3;
constexpr int EX_OFF = NSTAGE * STAGE;
constexpr int EX_Q = EX_OFF, EX_K = EX_Q + 8 * 512, EX_V = EX_K + 9 * 512;
constexpr int K1_PF = EX_V + 9 * 512;
constexpr int K1_LDS = K1_PF + 256;
constexpr int NT1 = 10;

__device__ __forceinline__ unsigned pack2(float a, float b) {
  f32x2 v = {a, b};
  bf16x2 r = __builtin_convertvector(v, bf16x2);
  return __builtin_bit_cast(unsigned, r);
}
__device__ __forceinline__ float bf_lo(unsigned u) { return __builtin_bit_cast(float, u << 16); }
__device__ __forceinline__ float bf_hi(unsigned u) { return __builtin_bit_cast(float, u & 0xffff0000u); }
__device__ __forceinline__ f32x4 mfma16(u32x4 a, u32x4 b, f32x4 c) {
  return __builtin_amdgcn_mfma_f32_16x16x32_bf16(__builtin_bit_cast(bf16x8, a), __builtin_bit_cast(bf16x8, b), c, 0, 0, 0);
}
__device__ __forceinline__ u32x4 lds_read128(unsigned addr) { return *(const LAS u32x4*)(uintptr_t)addr; }
__device__ __forceinline__ u32x2 lds_read64(unsigned addr) { return *(const LAS u32x2*)(uintptr_t)addr; }
__device__ __forceinline__ u32x2 lds_tr(unsigned addr) {
  s16x4 v = __builtin_amdgcn_ds_read_tr16_b64_v4i16((LAS s16x4*)(uintptr_t)addr);
  return __builtin_bit_cast(u32x2, v);
}
__device__ __forceinline__ u32x4 lds_wfrag(unsigned addr) {
  u32x2 a = lds_tr(addr), b = lds_tr(addr + 8 * W_ROW);
  u32x4 r = {a.x, a.y, b.x, b.y};
  return r;
}
__device__ __forceinline__ void dma16(unsigned ldsaddr, unsigned voff, u32x4 rsrc, unsigned soff) {
  asm volatile("s_mov_b32 m0, %0\n\ts_nop 0\n\tbuffer_load_dwordx4 %1, %2, %3 offen lds"
               :: "s"(ldsaddr), "v"(voff), "s"(rsrc), "s"(soff) : "memory");
}
#define PF_FLAGS ""
__device__ __forceinline__ void dma_touch(unsigned ldsaddr, unsigned voff, u32x4 rsrc, unsigned soff) {
  asm volatile("s_mov_b32 m0, %0\n\ts_nop 0\n\tbuffer_load_dword %1, %2, %3 offen " PF_FLAGS " lds"
               :: "s"(ldsaddr), "v"(voff), "s"(rsrc), "s"(soff) : "memory");
}
#define ST_FLAGS "sc1"
__device__ __forceinline__ void st16(void* p, u32x4 v) {
  asm volatile("global_store_dwordx4 %0, %1, off " ST_FLAGS "\n\ts_nop 1" :: "v"(p), "v"(v) : "memory");
}
__device__ __forceinline__ void st8(void* p, u32x2 v) {
  asm volatile("global_store_dwordx2 %0, %1, off " ST_FLAGS :: "v"(p), "v"(v) : "memory");
}
#define ST_AUX 16
typedef __amdgpu_buffer_rsrc_t rsrc_t;
__device__ __forceinline__ rsrc_t out_rsrc(void* p, unsigned bytes) { return __builtin_amdgcn_make_buffer_rsrc(p, 0, bytes, 0x00020000); }
template <int AUX> __device__ __forceinline__ void bst16(rsrc_t r, unsigned voff, u32x4 v) { __builtin_amdgcn_raw_buffer_store_b128(v, r, voff, 0, AUX); }
template <int AUX> __device__ __forceinline__ void bst8(rsrc_t r, unsigned voff, unsigned soff, u32x2 v) { __builtin_amdgcn_raw_buffer_store_b64(v, r, voff, soff, AUX); }
#define WAIT_V(n) asm volatile("s_waitcnt vmcnt(" #n ")" ::: "memory")
#define WAIT_L0() __builtin_amdgcn_s_waitcnt(0xC07F)
#define OPAQUE(x) asm volatile("" : "+v"(x))
#define BAR() __builtin_amdgcn_s_barrier()
#define SCHED() __builtin_amdgcn_sched_barrier(0)

__device__ __forceinline__ u32x4 make_rsrc(const void* p, unsigned bytes) {
  uint64_t a = (uint64_t)p;
  u32x4 r;
  r.x = __builtin_amdgcn_readfirstlane((unsigned)a);
  r.y = __builtin_amdgcn_readfirstlane((unsigned)(a >> 32) & 0xffffu);
  r.z = bytes;
  r.w = 0x00020000u;
  return r;
}

__device__ __forceinline__ int swap23(int k) { return (k & ~12) | ((k & 4) << 1) | ((k & 8) >> 1); }

struct Stager {
  unsigned vx, vw[7], v4;
  u32x4 rx, rw, r4;
  unsigned sx, sw, s4;
  unsigned wstep, step4;
  unsigned lx, lw, l4;
};
__device__ __forceinline__ void stage_x(const Stager& st, unsigned sb) {
#pragma unroll
  for (int p = 0; p < 4; ++p) dma16(sb + st.lx + p * 4096, st.vx, st.rx, st.sx + p * (32 * D * 2));
}
__device__ __forceinline__ void stage_w(const Stager& st, unsigned sb) {
#pragma unroll
  for (int j = 0; j < 7; ++j) dma16(sb + st.lw + j * 4096, st.vw[j], st.rw, st.sw);
  dma16(sb + st.l4, st.v4, st.r4, st.s4);
}
__device__ __forceinline__ void stage_next(Stager& st) { st.sx += 128u; st.sw += st.wstep; st.s4 += st.step4; }
__device__ __forceinline__ unsigned w_src(int wi, int lane, unsigned rowbytes, unsigned secstride, int chmax) {
  unsigned ci = wi * 64 + lane;
  unsigned pos = (ci * 2185u) >> 16, ch = ci - pos * 30u;
  unsigned sec = (ch * 205u) >> 11;
  unsigned k = swap23(pos);
  ch = ch < (unsigned)chmax ? ch : (unsigned)chmax;
  return k * rowbytes + ch * 16u + sec * secstride;
}

struct AttnAcc { f32x4 q[8], k[9], v[9], e[9]; };
__device__ __forceinline__ u32x4 cat(u32x2 a, u32x2 b) { return u32x4{a.x, a.y, b.x, b.y}; }

template <int W>
struct AttnOps {
  struct Frags { u32x4 x[9]; u32x2 wl[4], wh[4]; };
  AttnAcc a;
  unsigned xa, wa, we;
  static constexpr int per_rb(int rb) { return 2 + (rb < 8 ? 1 : 0) + ((W == 3 || (W == 0 && rb == 8)) ? 0 : 1); }
  static constexpr int NM = per_rb(8) + 8 * per_rb(0);
  static constexpr int NR = W < 3 ? 17 : 15;
  template <int I> __device__ __forceinline__ void mma(const Frags& f) {
    constexpr int rb = I < per_rb(8) ? 8 : (I - per_rb(8)) / per_rb(0);
    constexpr int j = I < per_rb(8) ? I : (I - per_rb(8)) % per_rb(0);
    constexpr bool has_e = !(W == 3 || (W == 0 && rb == 8));
    if constexpr (j == 0) a.k[rb] = mfma16(cat(f.wl[1], f.wh[1]), f.x[rb], a.k[rb]);
    else if constexpr (j == 1) a.v[rb] = mfma16(f.x[rb], cat(f.wl[2], f.wh[2]), a.v[rb]);
    else if constexpr (j == 2 && has_e) {
      if constexpr (W == 2) a.e[rb] = mfma16(f.x[rb], cat(f.wl[3], f.wh[3]), a.e[rb]);
      else a.e[rb] = mfma16(cat(f.wl[3], f.wh[3]), f.x[rb], a.e[rb]);
    } else a.q[rb] = mfma16(cat(f.wl[0], f.wh[0]), f.x[rb], a.q[rb]);
  }
  unsigned x, w, e;
  __device__ __forceinline__ void bases(unsigned sb, int kk) {
    x = (xa ^ (kk * 64)) + sb; w = wa + sb + kk * 32 * W_ROW; e = we + sb + kk * 32 * W_ROW;
    OPAQUE(x); OPAQUE(w); if constexpr (W < 3) OPAQUE(e);
  }
  template <int I0> __device__ __forceinline__ void read(Frags& f) {
    constexpr int I = (W == 3 && I0 >= 5) ? I0 + 2 : I0;
    if constexpr (I == 0) f.wl[1] = lds_tr(w + 160);
    if constexpr (I == 1) f.wh[1] = lds_tr(w + 160 + 8 * W_ROW);
    if constexpr (I == 2) f.x[8] = lds_read128(x + 8 * 2048);
    if constexpr (I == 3) f.wl[2] = lds_tr(w + 320);
    if constexpr (I == 4) f.wh[2] = lds_tr(w + 320 + 8 * W_ROW);
    if constexpr (I == 5) f.wl[3] = lds_tr(e);
    if constexpr (I == 6) f.wh[3] = lds_tr(e + 8 * W_ROW);
    if constexpr (I == 7) f.x[0] = lds_read128(x);
    if constexpr (I == 8) f.wl[0] = lds_tr(w);
    if constexpr (I == 9) f.wh[0] = lds_tr(w + 8 * W_ROW);
    if constexpr (I >= 10) f.x[I - 9] = lds_read128(x + (I - 9) * 2048);
  }
};

struct MlpAcc { f32x4 c[4][7]; };
struct MlpOps {
  struct Frags { u32x4 x[4]; u32x2 wl[7], wh[7]; };
  MlpAcc a;
  unsigned xa, wa;
  static constexpr int NM = 28, NR = 18;
  template <int I> __device__ __forceinline__ void mma(const Frags& f) {
    constexpr int n = I / 4, r = I % 4;
    a.c[r][n] = mfma16(cat(f.wl[n], f.wh[n]), f.x[r], a.c[r][n]);
  }
  unsigned x, w, w6, d6;
  __device__ __forceinline__ void bases(unsigned sb, int kk) {
    x = (xa ^ (kk * 64)) + sb; w = wa + sb + kk * 32 * W_ROW; w6 = w + d6;
    OPAQUE(x); OPAQUE(w); OPAQUE(w6);
  }
  template <int I> __device__ __forceinline__ void read(Frags& f) {
    if constexpr (I < 2) { if constexpr (I == 0) f.wl[0] = lds_tr(w); else f.wh[0] = lds_tr(w + 8 * W_ROW); }
    else if constexpr (I < 6) f.x[I - 2] = lds_read128(x + (I - 2) * 2048);
    else {
      constexpr int n = 1 + (I - 6) / 2;
      const unsigned a = n < 6 ? w + 32 * n : w6;
      if constexpr ((I & 1) == 0) f.wl[n] = lds_tr(a); else f.wh[n] = lds_tr(a + 8 * W_ROW);
    }
  }
};

template <class OPS, int I>
__device__ __forceinline__ void read_all(OPS& o, typename OPS::Frags& f) {
  if constexpr (I < OPS::NR) { o.template read<I>(f); read_all<OPS, I + 1>(o, f); }
}
template <class OPS, int I>
__device__ __forceinline__ void half_step(OPS& o, const typename OPS::Frags& fc, typename OPS::Frags& fn) {
  if constexpr (I < OPS::NM) {
    o.template mma<I>(fc);
#define RD_TAIL 6
    constexpr int span = OPS::NM - RD_TAIL;
    static_assert(span >= OPS::NR, "one read per gap");
    constexpr int j = (I * OPS::NR + span - 1) / span;
    if constexpr (j < OPS::NR && j * span / OPS::NR == I) { SCHED(); o.template read<j>(fn); }
    SCHED();
    half_step<OPS, I + 1>(o, fc, fn);
  }
}

template <class OPS>
__device__ __forceinline__ void k_loop(OPS& o, int live) {
  typename OPS::Frags f0, f1;
  o.bases(0, 0);
  if (live) BAR();
  read_all<OPS, 0>(o, f0);
  unsigned sb = 0;
#pragma unroll 1
  for (int t = live ? 0 : NT1 - 1; t < NT1; ++t) {
    unsigned sn = sb + STAGE; if (sn == NSTAGE * STAGE) sn = 0;
    o.bases(sb, 1);
    WAIT_L0();
    SCHED();
    half_step<OPS, 0>(o, f0, f1);
    o.bases(sn, 0);
    WAIT_L0();
    if (t < NT1 - 1) BAR();
    SCHED();
    half_step<OPS, 0>(o, f1, f0);
    sb = sn;
  }
}

__device__ __forceinline__ float fmx(float a, float b) { return __builtin_elementwise_maximum(a, b); }
__device__ __forceinline__ void swap32(float& a, float& b) {
  auto r = __builtin_amdgcn_permlane32_swap(__builtin_bit_cast(unsigned, a), __builtin_bit_cast(unsigned, b), false, false);
  unsigned r0 = r[0], r1 = r[1];
  a = __builtin_bit_cast(float, r0); b = __builtin_bit_cast(float, r1);
}
__device__ __forceinline__ void swap16(float& a, float& b) {
  auto r = __builtin_amdgcn_permlane16_swap(__builtin_bit_cast(unsigned, a), __builtin_bit_cast(unsigned, b), false, false);
  unsigned r0 = r[0], r1 = r[1];
  a = __builtin_bit_cast(float, r0); b = __builtin_bit_cast(float, r1);
}
template <bool MAX>
__device__ __forceinline__ float reduce4(float x0, float x1, float x2, float x3) {
  swap32(x0, x1); swap32(x2, x3);
  float y01 = MAX ? fmx(x0, x1) : x0 + x1, y23 = MAX ? fmx(x2, x3) : x2 + x3;
  swap16(y01, y23);
  return MAX ? fmx(y01, y23) : y01 + y23;
}
__device__ __forceinline__ void bcast4(float z, float& x0, float& x1, float& x2, float& x3) {
  float u = z, w = z;
  swap16(u, w);
  x0 = u; x1 = u; swap32(x0, x1);
  x2 = w; x3 = w; swap32(x2, x3);
}

struct AttnPacked { u32x2 q[8], k[9], v[9]; };

template <int W>
__device__ __forceinline__ void attn_main(int lane, int live, AttnPacked& pk, unsigned char* __restrict__ ws) {
  OPAQUE(lane);
  const int c = lane & 15, g = lane >> 4, q4 = (lane >> 2) & 3, p4 = lane & 3;
  AttnOps<W> o;
#pragma unroll
  for (int i = 0; i < 9; ++i) {
    if (i < 8) o.a.q[i] = f32x4{0.f, 0.f, 0.f, 0.f};
    o.a.k[i] = f32x4{0.f, 0.f, 0.f, 0.f}; o.a.v[i] = f32x4{0.f, 0.f, 0.f, 0.f}; o.a.e[i] = f32x4{0.f, 0.f, 0.f, 0.f};
  }
  o.xa = c * 128 + ((g ^ ((c >> 1) & 7)) * 16);
  const unsigned wrow = X_BYTES + ((g >> 1) * 16 + (g & 1) * 4 + q4) * W_ROW;
  o.wa = wrow + 40 * W + 8 * p4;
  o.we = wrow + 160 * (W < 3 ? W : 0) + 40 * p4 + 32;
  STAMP(ws, 0, 1);
  k_loop(o, live);
  STAMP(ws, 0, 2);
  OPAQUE(lane);
  if constexpr (W < 3) {
    constexpr int base = W == 0 ? EX_Q : (W == 1 ? EX_K : EX_V);
#pragma unroll
    for (int i = 0; i < (W == 0 ? 8 : 9); ++i) {
      u32x2 v = {pack2(o.a.e[i][0], o.a.e[i][1]), pack2(o.a.e[i][2], o.a.e[i][3])};
      *(LAS u32x2*)(uintptr_t)(base + i * 512 + lane * 8) = v;
    }
  }
#pragma unroll
  for (int i = 0; i < 9; ++i) {
    if (i < 8) pk.q[i] = u32x2{pack2(o.a.q[i][0], o.a.q[i][1]), pack2(o.a.q[i][2], o.a.q[i][3])};
    pk.k[i] = u32x2{pack2(o.a.k[i][0], o.a.k[i][1]), pack2(o.a.k[i][2], o.a.k[i][3])};
    pk.v[i] = u32x2{pack2(o.a.v[i][0], o.a.v[i][1]), pack2(o.a.v[i][2], o.a.v[i][3])};
  }
}

__device__ __forceinline__ void attn_epilogue(int lane, int live, int W, int mt, int hg, const AttnPacked& pk, unsigned char* __restrict__ ws) {
  OPAQUE(lane);
  const int c = lane & 15, g = lane >> 4;
  f32x4 mprev, mcur, mfirst;
#pragma unroll
  for (int r = 0; r < 4; ++r) {
    int dcur = c - 4 * g - r, dprev = dcur + 16;
    mcur[r] = (dcur >= 0 && (dcur & 1) == 0) ? 0.f : -1e30f;
    mprev[r] = (dprev <= 14 && (dprev & 1) == 0) ? 0.f : -1e30f;
    mfirst[r] = mt == 0 ? -1e30f : mprev[r];
  }
  const float c2 = 0.22360679774997896f * 1.4426950408889634f;
  const unsigned sel_lo = (c & 1) ? 0x05040c0cu : 0x0c0c0504u, sel_hi = (c & 1) ? 0x07060c0cu : 0x0c0c0706u;
  unsigned exa = EX_OFF + W * 128 + c * 8 + g * 2, exv = EX_V + lane * 8;
  OPAQUE(exa); OPAQUE(exv);
  WAIT_L0();
  if (live) BAR();
  unsigned kx[9], qx[8]; u32x2 vx[9];
#pragma unroll
  for (int i = 0; i < 9; ++i) {
    kx[i] = *(const LAS unsigned short*)(uintptr_t)(exa + (EX_K - EX_OFF) + i * 512);
    if (i < 8) qx[i] = *(const LAS unsigned short*)(uintptr_t)(exa + (EX_Q - EX_OFF) + i * 512);
  }
#pragma unroll
  for (int i = 0; i < 9; ++i) vx[i] = lds_read64(exv + i * 512);
  float t[8][4], m[8];
  f32x4 sp_[8], sc_[8];
#pragma unroll
  for (int i = 0; i < 8; ++i) {
    const int ip = i == 0 ? 8 : i - 1;
    const u32x4 qf = {pk.q[i].x, pk.q[i].y, qx[i], 0u};
    sp_[i] = mfma16(u32x4{pk.k[ip].x, pk.k[ip].y, kx[ip], 0u}, qf, i == 0 ? mfirst : mprev);
    sc_[i] = mfma16(u32x4{pk.k[i].x, pk.k[i].y, kx[i], 0u}, qf, mcur);
  }
  __builtin_amdgcn_sched_group_barrier(0x008, 4, 0);
#pragma unroll
  for (int i = 0; i < 12; ++i) { __builtin_amdgcn_sched_group_barrier(0x008, 1, 0); __builtin_amdgcn_sched_group_barrier(0x002, 3, 0); }
#pragma unroll
  for (int i = 0; i < 8; ++i) {
    const f32x4 sp = sp_[i], sc = sc_[i];
    t[i][0] = fmx(sp[0], sp[1]); t[i][1] = fmx(sp[2], sp[3]); t[i][2] = fmx(sc[0], sc[1]); t[i][3] = fmx(sc[2], sc[3]);
    m[i] = fmx(fmx(t[i][0], t[i][1]), fmx(t[i][2], t[i][3]));
  }
  bcast4(reduce4<true>(m[0], m[1], m[2], m[3]) * -c2, m[0], m[1], m[2], m[3]);
  bcast4(reduce4<true>(m[4], m[5], m[6], m[7]) * -c2, m[4], m[5], m[6], m[7]);
  float l[8];
#pragma unroll
  for (int i = 0; i < 8; ++i) {
#pragma unroll
    for (int r = 0; r < 4; ++r) t[i][r] = __builtin_amdgcn_exp2f(fmaf(t[i][r], c2, m[i]));
    l[i] = (t[i][0] + t[i][1]) + (t[i][2] + t[i][3]);
  }
  bcast4(__builtin_amdgcn_rcpf(reduce4<false>(l[0], l[1], l[2], l[3])), l[0], l[1], l[2], l[3]);
  bcast4(__builtin_amdgcn_rcpf(reduce4<false>(l[4], l[5], l[6], l[7])), l[4], l[5], l[6], l[7]);
  const int H = hg * 4 + W;
  const rsrc_t ro = out_rsrc(ws + A2_OFF, live ? (unsigned)A2_BYTES : 0u);
  const unsigned am = (mt * 2 * KS2 + (H >> 1)) * 4096 + (H & 1) * 8;
  const unsigned ax = (mt * 2 * KS2 + 16 + (hg >> 1)) * 4096 + (hg & 1) * 8;
  const unsigned vo = lane * 16, vox = g == W ? vo : 0x40000000u;
#pragma unroll
  for (int i = 0; i < 8; ++i) {
    const int ip = i == 0 ? 8 : i - 1;
    const unsigned p01 = pack2(t[i][0] * l[i], t[i][1] * l[i]), p23 = pack2(t[i][2] * l[i], t[i][3] * l[i]);
    const u32x4 pf = {__builtin_amdgcn_perm(p01, 0u, sel_lo), __builtin_amdgcn_perm(p01, 0u, sel_hi),
                      __builtin_amdgcn_perm(p23, 0u, sel_lo), __builtin_amdgcn_perm(p23, 0u, sel_hi)};
    const f32x4 z = {0.f, 0.f, 0.f, 0.f};
    f32x4 om = mfma16(cat(pk.v[ip], pk.v[i]), pf, z);
    f32x4 ox = mfma16(cat(vx[ip], vx[i]), pf, z);
    bst8<0>(ro, vo + (i & 3) * 1024, am + (i >> 2) * (KS2 * 4096), u32x2{pack2(om[0], om[1]), pack2(om[2], om[3])});
    bst8<0>(ro, vox + (i & 3) * 1024, ax + (i >> 2) * (KS2 * 4096), u32x2{pack2(ox[0], ox[1]), pack2(ox[2], ox[3])});
  }
}

__device__ __forceinline__ unsigned mish2(float h0, float h1) {
  f32x2 h = {h0, h1};
  f32x2 a = h * 1.4426950408889634f;
  f32x2 w = {__builtin_amdgcn_exp2f(a.x), __builtin_amdgcn_exp2f(a.y)};
  w = w + 1.f;
  f32x2 d = __builtin_elementwise_fma(w, w, f32x2{1.f, 1.f});
  f32x2 r = {__builtin_amdgcn_rcpf(d.x), __builtin_amdgcn_rcpf(d.y)};
  f32x2 t = __builtin_elementwise_fma(r, f32x2{-2.f, -2.f}, f32x2{1.f, 1.f});
  f32x2 y = h * t;
  return pack2(y.x, y.y);
}

__device__ __forceinline__ void mlp_role(int lane, int live, int wave, int mt, int nb0, const u32x2 (&bias)[7],
                                         unsigned char* __restrict__ ws) {
  const int c = lane & 15, g = lane >> 4, q4 = (lane >> 2) & 3, p4 = lane & 3;
  const int wm = wave >> 1, wn = wave & 1;
  MlpOps o;
#pragma unroll
  for (int n = 0; n < 7; ++n) {
    f32x4 b = {bf_lo(bias[n].x), bf_hi(bias[n].x), bf_lo(bias[n].y), bf_hi(bias[n].y)};
#pragma unroll
    for (int r = 0; r < 4; ++r) o.a.c[r][n] = b;
  }
  o.xa = wm * 4 * 2048 + c * 128 + ((g ^ ((c >> 1) & 7)) * 16);
  o.wa = X_BYTES + ((g >> 1) * 16 + (g & 1) * 4 + q4) * W_ROW + wn * 8 * 32 + 8 * p4;
  o.d6 = wn ? (unsigned)-32 : 192u;
  STAMP(ws, 0, 1);
  k_loop(o, live);
  STAMP(ws, 0, 2);

  const rsrc_t ro = out_rsrc(ws + A2_OFF, live ? (unsigned)A2_BYTES : 0u);
  const unsigned so = ((mt * 2 + wm) * KS2 + 20 + (nb0 >> 1)) * 4096;
  const unsigned vo = lane * 16;
  unsigned vj[3];
#pragma unroll
  for (int j = 0; j < 3; ++j) { vj[j] = vo + so + (wn * 4 + j) * 4096; OPAQUE(vj[j]); }
#pragma unroll
  for (int r = 0; r < 4; ++r) {
#pragma unroll
    for (int j = 0; j < 3; ++j) {
      const f32x4 h0 = o.a.c[r][2 * j], h1 = o.a.c[r][2 * j + 1];
      bst16<ST_AUX>(ro, vj[j] + r * 1024,
                    u32x4{mish2(h0[0], h0[1]), mish2(h0[2], h0[3]), mish2(h1[0], h1[1]), mish2(h1[2], h1[3])});
    }
    const f32x4 h = o.a.c[r][6];
    bst8<0>(ro, vo + r * 1024, so + 3 * 4096 + wn * 8, u32x2{mish2(h[0], h[1]), mish2(h[2], h[3])});
  }
}

__device__ __forceinline__ int w2cat_row(int beta, int rho) {
  if (beta < 32) return 20 * beta + rho;
  if (beta < 40) return 20 * (4 * (beta - 32) + (rho >> 2)) + 16 + (rho & 3);
  return 16 * (beta - 40) + rho;
}

__global__ __launch_bounds__(512, 2) void k1_kernel(const unsigned short* __restrict__ x, const unsigned short* __restrict__ wqkv,
                                                    const unsigned short* __restrict__ wo, const unsigned short* __restrict__ w1,
                                                    const unsigned short* __restrict__ b1, const unsigned short* __restrict__ w2,
                                                    const unsigned short* __restrict__ gate_w, unsigned char* __restrict__ ws) {
  extern __shared__ __attribute__((aligned(16))) unsigned char smem[];
  const int lane = threadIdx.x & 63;
  const int wave = __builtin_amdgcn_readfirstlane(threadIdx.x >> 6);
  const int b = blockIdx.x, xcd = b & 7, idx = b >> 3;
  const int mt = (xcd & 1) * 8 + (idx & 7);
  const int nsel = idx >> 3;
  const int nt = (xcd >> 1) * 2 + (nsel & 1);
  const bool attn = nsel < 2;
  const int m0 = mt * 128;
  const int nb0 = nt * 14 < 94 ? nt * 14 : 94;
  STAMP(ws, 0, 0);

  if (wave >= 4) {
    const int lw = wave - 4;
    Stager st;
    st.rx = make_rsrc(x, S_TOK * D * 2);
    st.vx = (unsigned)((8 * lw + (lane >> 3)) * (D * 2) + (((lane & 7) ^ ((4 * lw + (lane >> 4)) & 7)) * 16));
    st.sx = (unsigned)(m0 * D * 2);
    st.lx = lw * 1024;
    stage_x(st, 0);
    st.rw = make_rsrc(attn ? wqkv : w1, attn ? D * 3 * D * 2 : D * F * 2);
    const unsigned rowbytes = attn ? 3 * D * 2 : F * 2, secstride = attn ? D * 2 - 160 : 0;
    const int chmax = attn ? 29 : 27;
    st.wstep = 64u * rowbytes;
    st.sw = attn ? nt * 160 : nb0 * 32;
    st.lw = X_BYTES + lw * 1024;
#pragma unroll
    for (int j = 0; j < 7; ++j) st.vw[j] = w_src(lw + 4 * j, lane, rowbytes, secstride, chmax);
    {
      unsigned v4w = w_src(26 + lw, lane, rowbytes, secstride, chmax);
      const bool hx = lw < 2;
      st.v4 = hx ? st.vx : v4w;
      st.r4 = hx ? st.rx : st.rw;
      st.s4 = hx ? (m0 ? st.sx - 16 * D * 2 : 0u) : st.sw;
      st.step4 = hx ? 128u : st.wstep;
      st.l4 = hx ? (16 + lw) * 1024 : X_BYTES + (26 + lw) * 1024;
    }
    stage_w(st, 0); stage_next(st);
    stage_x(st, STAGE); stage_w(st, STAGE); stage_next(st);
    WAIT_V(12);
    STAMP(ws, 0, 1);
    BAR();
    stage_x(st, 2 * STAGE); stage_w(st, 2 * STAGE); stage_next(st);
    unsigned sb = 0;
#pragma unroll 1
    for (int t = 0; t < NT1 - 3; ++t) {
      WAIT_V(12);
      BAR();
      stage_x(st, sb); stage_w(st, sb); stage_next(st);
      sb += STAGE; if (sb == NSTAGE * STAGE) sb = 0;
    }
    WAIT_V(12); BAR();
    const int unit = lw * 256 + b;
    const bool tile = unit < KS2 * 8;
    const int ks = unit >> 3, ct = unit & 7;
    const unsigned lt = STAGE + lw * 5120;
    {
      const u32x4 r2 = make_rsrc(ks < 20 ? wo : w2, tile ? (ks < 20 ? D * D * 2 : F * D * 2) : 0);
#pragma unroll
      for (int i = 0; i < 5; ++i) {
        unsigned ci = i * 64 + lane, row = (ci * 205u) >> 11, ch = ci - row * 10u;
        dma16(lt + i * 1024, (unsigned)w2cat_row(2 * ks + (row >> 4), row & 15) * (D * 2) + ch * 16, r2, ct * 160);
      }
    }
    WAIT_V(5); BAR();
    const int tok = m0 + ((attn ? 0 : 8) + nt) * 8 + lw * 2 + (lane >> 5);
    {
      const u32x2* xr = (const u32x2*)(x + (size_t)tok * D) + (lane & 31);
      const u32x2* gp = (const u32x2*)gate_w + (lane & 31);
      float sg = 0.f;
#pragma unroll
      for (int j = 0; j < 5; ++j) {
        u32x2 a = xr[32 * j], w = gp[32 * j];
        sg = fmaf(bf_lo(a.x), bf_lo(w.x), sg); sg = fmaf(bf_hi(a.x), bf_hi(w.x), sg);
        sg = fmaf(bf_lo(a.y), bf_lo(w.y), sg); sg = fmaf(bf_hi(a.y), bf_hi(w.y), sg);
      }
#pragma unroll
      for (int o = 16; o >= 1; o >>= 1) sg += __shfl_xor(sg, o, 64);
      if ((lane & 31) == 0) ((float*)(ws + G_OFF))[tok] = 1.f / (1.f + __expf(-sg));
    }
    WAIT_V(1);
    if (tile) {
      const unsigned ra = lt + (4 * (lane >> 4) + ((lane >> 2) & 3)) * 160 + (lane & 3) * 40;
      unsigned char* bp = ws + B2_OFF + ((size_t)ct * KS2 + ks) * 5120 + lane * 16;
#pragma unroll
      for (int i = 0; i < 5; ++i) st16(bp + i * 1024, cat(lds_tr(ra + 8 * i), lds_tr(ra + 8 * i + 16 * 160)));
    }
    if (attn) BAR();
    STAMP(ws, 0, 3);
    return;
  }
  if (!attn) {
    const int pw = (b - 128) * 4 + wave;
    const int li = pw * 28 + (lane < 28 ? lane : 0);
    dma_touch(K1_PF, (unsigned)((li / 7) * (D * 2) + (3 + li % 7) * 128), make_rsrc(x, S_TOK * D * 2), 0);
    {
      const int q = ((idx - 16) * 4 + wave) * 63 + (lane < 63 ? lane : 0), row = q / 9, sl = q - row * 9;
      dma_touch(K1_PF, (unsigned)(row * (3 * D * 2) + (sl / 3) * (D * 2) + (sl % 3) * 128 + (xcd >> 1) * 320), make_rsrc(wqkv, D * 3 * D * 2), 192 * 3 * D * 2);
    }
    dma_touch(K1_PF, (unsigned)((pw * 24 + (lane < 24 ? lane : 0)) * 128), make_rsrc(w1, D * F * 2), 192 * F * 2);
  }
#define PASS0 1
  if (attn) {
#pragma nounroll
    for (int live = PASS0; live < 2; ++live) {
      asm volatile("" : "+s"(live));
      AttnPacked pk;
      if (wave == 0) attn_main<0>(lane, live, pk, ws);
      else if (wave == 1) attn_main<1>(lane, live, pk, ws);
      else if (wave == 2) attn_main<2>(lane, live, pk, ws);
      else attn_main<3>(lane, live, pk, ws);
      attn_epilogue(lane, live, wave, mt, nt, pk, ws);
    }
  } else {
    u32x2 bias[7];
    const u32x2* bp = (const u32x2*)(b1 + nb0 * 16 + (lane >> 4) * 4);
#pragma unroll
    for (int n = 0; n < 7; ++n) bias[n] = bp[(n < 6 ? (wave & 1) * 8 + n : 6 + (wave & 1)) * 4];
#pragma nounroll
    for (int live = PASS0; live < 2; ++live) {
      asm volatile("" : "+s"(live));
      mlp_role(lane, live, wave, mt, nb0, bias, ws);
    }
  }
  STAMP(ws, 0, 3);
}

constexpr int K2_STG = 36 * 1024, K2_NSLOT = 4, K2_NST = 20;
constexpr int K2_RING = K2_STG * K2_NSLOT, K2_LDS = K2_RING + 256;
#define K2_PFD 4

struct K2Frags { u32x4 a[2][2], b[2][5]; };
template <int I>
__device__ __forceinline__ void k2_step(f32x4 (&acc)[2][5], const K2Frags& fc, K2Frags& fn, unsigned ra, unsigned rb) {
  if constexpr (I < 20) {
    constexpr int k = I / 10, i = (I % 10) / 5, j = I % 5;
    acc[i][j] = mfma16(fc.b[k][j], fc.a[k][i], acc[i][j]);
    SCHED();
    if constexpr (I < 14) {
      constexpr int kk = I / 7, r = I % 7;
      if constexpr (r < 2) fn.a[kk][r] = lds_read128(ra + r * 1024 + kk * 9216);
      else fn.b[kk][r - 2] = lds_read128(rb + (r - 2) * 1024 + kk * 9216);
      SCHED();
    }
    k2_step<I + 1>(acc, fc, fn, ra, rb);
  }
}

__global__ __launch_bounds__(512, 2) void k2_kernel(const unsigned char* __restrict__ ws, unsigned short* __restrict__ out) {
  extern __shared__ __attribute__((aligned(16))) unsigned char smem[];
  const int lane = threadIdx.x & 63;
  const int wave = __builtin_amdgcn_readfirstlane(threadIdx.x >> 6);
  const int b = blockIdx.x, xcd = b & 7, idx = b >> 3;
  const int mt = (xcd & 3) * 8 + (idx & 7);
  const int nt = (xcd >> 2) * 4 + (idx >> 3);
  STAMP(ws, 1, 0);
  if (wave >= 4) {
    const int lw = wave - 4;
    const u32x4 rs = make_rsrc(ws, (unsigned)(A2_BYTES + B2_BYTES));
    const unsigned v = lane * 16;
    unsigned sa = (unsigned)A2_OFF + (mt * KS2 + lw) * 4096, sbb = (unsigned)B2_OFF + (nt * KS2 + lw) * 5120;
    unsigned l = lw * 9 * 1024;
    auto issue = [&](bool live) {
      const unsigned oob = live ? 0u : 0x40000000u;
#pragma unroll
      for (int i = 0; i < 4; ++i) dma16(l + i * 1024, v, rs, sa + i * 1024 + oob);
#pragma unroll
      for (int j = 0; j < 5; ++j) dma16(l + (4 + j) * 1024, v, rs, sbb + j * 1024 + oob);
      sa += 4 * 4096; sbb += 4 * 5120; l += K2_STG; if (l >= K2_RING) l -= K2_RING;
    };
    issue(true); issue(true); issue(true);
#pragma unroll 1
    for (int s = 0; s < K2_NST - 3; ++s) {
      WAIT_V(18);
      BAR();
      issue(4 * (s + 3) + lw < KS2);
    }
    WAIT_V(18); BAR();
    WAIT_V(9); BAR();
    WAIT_V(0); BAR();
    return;
  }
  const int c = lane & 15, g = lane >> 4;
  const int kh = wave >> 1, tp = wave & 1;
  const float* gp = (const float*)(ws + G_OFF) + mt * 64 + tp * 32 + c;
  const float gate0 = gp[0], gate1 = gp[16];
  f32x4 acc[2][5], res[2][5];
  K2Frags f0, f1;
#pragma unroll
  for (int j = 0; j < 5; ++j) {
    acc[0][j] = acc[1][j] = f32x4{0.f, 0.f, 0.f, 0.f};
    f1.b[0][j] = f1.b[1][j] = u32x4{0u, 0u, 0u, 0u};
  }
  f1.a[0][0] = f1.a[0][1] = f1.a[1][0] = f1.a[1][1] = u32x4{0u, 0u, 0u, 0u};
  const unsigned r0 = kh * 2 * 9216 + lane * 16;
  unsigned sl = 0;
  const u32x4 rs = make_rsrc(ws, (unsigned)(A2_BYTES + B2_BYTES));
  const unsigned pstep = tp ? 8 * 5120 : 8 * 4096;
  const unsigned pend = tp ? (unsigned)B2_OFF + (nt + 1) * KS2 * 5120 : (unsigned)A2_OFF + (mt + 1) * KS2 * 4096;
  unsigned pv = tp ? (unsigned)B2_OFF + nt * KS2 * 5120 + (idx & 7) * 5120 + (lane < 40 ? lane : 0) * 128
                   : (unsigned)A2_OFF + mt * KS2 * 4096 + (idx >> 3) * 8192 + lane * 128;
#pragma unroll 1
  for (int it = 0; it < K2_NST / 2; ++it) {
    unsigned ra = sl + r0 + tp * 2048, rb = sl + r0 + 4096;
    OPAQUE(ra); OPAQUE(rb);
    if (it == 3) {
#pragma unroll
      for (int j = 0; j < 5; ++j) {
        res[0][j] = acc[0][j] * gate0; res[1][j] = acc[1][j] * gate1;
        acc[0][j] = acc[1][j] = f32x4{0.f, 0.f, 0.f, 0.f};
      }
    }
    BAR();
    SCHED();
    k2_step<0>(acc, f1, f0, ra, rb);
    WAIT_L0();
    BAR();
    SCHED();
    k2_step<0>(acc, f0, f1, ra + K2_STG, rb + K2_STG);
    WAIT_L0();
    sl ^= 2 * K2_STG;
  }
  STAMP(ws, 1, 1);
  f32x4 keep[5];
#pragma unroll
  for (int j = 0; j < 5; ++j) {
    f32x4 p0 = res[0][j] + acc[0][j] * (1.f - gate0), p1 = res[1][j] + acc[1][j] * (1.f - gate1);
    keep[j] = kh ? p1 : p0;
    *(LAS f32x4*)(uintptr_t)((wave * 5 + j) * 1024 + lane * 16) = kh ? p0 : p1;
  }
  WAIT_L0();
  BAR();
  unsigned* op = (unsigned*)(out + (size_t)(mt * 64 + (2 * tp + kh) * 16 + c) * D + nt * 80 + g * 20);
  unsigned o[10];
#pragma unroll
  for (int j = 0; j < 5; ++j) {
    f32x4 r = keep[j] + *(const LAS f32x4*)(uintptr_t)(((wave ^ 2) * 5 + j) * 1024 + lane * 16);
    o[2 * j] = pack2(r[0], r[1]); o[2 * j + 1] = pack2(r[2], r[3]);
  }
  typedef u32x4 __attribute__((aligned(8))) u32x4_a8;
  *(u32x4_a8*)op = u32x4{o[0], o[1], o[2], o[3]};
  *(u32x4_a8*)(op + 4) = u32x4{o[4], o[5], o[6], o[7]};
  *(u32x2*)(op + 8) = u32x2{o[8], o[9]};
  STAMP(ws, 1, 2);
}

}

extern "C" void kernel_launch(void* const* d_in, const int* in_sizes, int n_in, void* d_out, int out_size, void* d_ws,
                              size_t ws_size, hipStream_t stream) {
  const unsigned short* x = (const unsigned short*)d_in[0];
  const unsigned short* wqkv = (const unsigned short*)d_in[1];
  const unsigned short* wo = (const unsigned short*)d_in[2];
  const unsigned short* w1 = (const unsigned short*)d_in[4];
  const unsigned short* b1 = (const unsigned short*)d_in[5];
  const unsigned short* w2 = (const unsigned short*)d_in[6];
  const unsigned short* gate_w = (const unsigned short*)d_in[7];
  (void)hipFuncSetAttribute((const void*)k1_kernel, hipFuncAttributeMaxDynamicSharedMemorySize, K1_LDS);
  (void)hipFuncSetAttribute((const void*)k2_kernel, hipFuncAttributeMaxDynamicSharedMemorySize, K2_LDS);
  k1_kernel<<<256, 512, K1_LDS, stream>>>(x, wqkv, wo, w1, b1, w2, gate_w, (unsigned char*)d_ws);
  k2_kernel<<<256, 512, K2_LDS, stream>>>((const unsigned char*)d_ws, (unsigned short*)d_out);
}
```

```cpp
#include <hip/hip_runtime.h>
#include <hip/hip_bf16.h>
#include <stdint.h>

#define STAMP(ws, kern, i)
#define NO_PF2
#define PF_XCD
#define CODE_PF 0
#define CODE_BYTES 28000

namespace {

typedef __attribute__((ext_vector_type(4))) short s16x4;
typedef __attribute__((ext_vector_type(8))) __bf16 bf16x8;
typedef __attribute__((ext_vector_type(2))) __bf16 bf16x2;
typedef __attribute__((ext_vector_type(4))) float f32x4;
typedef __attribute__((ext_vector_type(2))) float f32x2;
typedef __attribute__((ext_vector_type(4))) unsigned u32x4;
typedef __attribute__((ext_vector_type(2))) unsigned u32x2;
#define LAS __attribute__((address_space(3)))

constexpr int S_TOK = 2048, D = 640, F = 1728;
constexpr int KS2 = 74;
constexpr size_t A2_BYTES = (size_t)128 * KS2 * 1024;
constexpr size_t B2_BYTES = (size_t)40 * KS2 * 1024;
constexpr size_t A2_OFF = 0, B2_OFF = A2_OFF + A2_BYTES, G_OFF = B2_OFF + B2_BYTES;

constexpr int X_BYTES = 144 * 128;
constexpr int W_ROW = 480;
constexpr int W_BYTES = 64 * W_ROW;
constexpr int STAGE = X_BYTES + W_BYTES;
constexpr int NSTAGE = 3;
constexpr int EX_OFF = NSTAGE * STAGE;
constexpr int EX_Q = EX_OFF, EX_K = EX_Q + 8 * 512, EX_V = EX_K + 9 * 512;
constexpr int K1_PF = EX_V + 9 * 512;
constexpr int K1_LDS = K1_PF + 256;
constexpr int NT1 = 10;

__device__ __forceinline__ unsigned pack2(float a, float b) {
  f32x2 v = {a, b};
  bf16x2 r = __builtin_convertvector(v, bf16x2);
  return __builtin_bit_cast(unsigned, r);
}
__device__ __forceinline__ float bf_lo(unsigned u) { return __builtin_bit_cast(float, u << 16); }
__device__ __forceinline__ float bf_hi(unsigned u) { return __builtin_bit_cast(float, u & 0xffff0000u); }
__device__ __forceinline__ f32x4 mfma16(u32x4 a, u32x4 b, f32x4 c) {
  return __builtin_amdgcn_mfma_f32_16x16x32_bf16(__builtin_bit_cast(bf16x8, a), __builtin_bit_cast(bf16x8, b), c, 0, 0, 0);
}
__device__ __forceinline__ u32x4 lds_read128(unsigned addr) { return *(const LAS u32x4*)(uintptr_t)addr; }
__device__ __forceinline__ u32x2 lds_read64(unsigned addr) { return *(const LAS u32x2*)(uintptr_t)addr; }
__device__ __forceinline__ u32x2 lds_tr(unsigned addr) {
  s16x4 v = __builtin_amdgcn_ds_read_tr16_b64_v4i16((LAS s16x4*)(uintptr_t)addr);
  return __builtin_bit_cast(u32x2, v);
}
__device__ __forceinline__ u32x4 lds_wfrag(unsigned addr) {
  u32x2 a = lds_tr(addr), b = lds_tr(addr + 8 * W_ROW);
  u32x4 r = {a.x, a.y, b.x, b.y};
  return r;
}
__device__ __forceinline__ void dma16(unsigned ldsaddr, unsigned voff, u32x4 rsrc, unsigned soff) {
  asm volatile("s_mov_b32 m0, %0\n\ts_nop 0\n\tbuffer_load_dwordx4 %1, %2, %3 offen lds"
               :: "s"(ldsaddr), "v"(voff), "s"(rsrc), "s"(soff) : "memory");
}
#define PF_FLAGS ""
__device__ __forceinline__ void dma_touch(unsigned ldsaddr, unsigned voff, u32x4 rsrc, unsigned soff) {
  asm volatile("s_mov_b32 m0, %0\n\ts_nop 0\n\tbuffer_load_dword %1, %2, %3 offen " PF_FLAGS " lds"
               :: "s"(ldsaddr), "v"(voff), "s"(rsrc), "s"(soff) : "memory");
}
#define ST_FLAGS "sc1"
__device__ __forceinline__ void st16(void* p, u32x4 v) {
  asm volatile("global_store_dwordx4 %0, %1, off " ST_FLAGS "\n\ts_nop 1" :: "v"(p), "v"(v) : "memory");
}
__device__ __forceinline__ void st8(void* p, u32x2 v) {
  asm volatile("global_store_dwordx2 %0, %1, off " ST_FLAGS :: "v"(p), "v"(v) : "memory");
}
#define ST_AUX 16
typedef __amdgpu_buffer_rsrc_t rsrc_t;
__device__ __forceinline__ rsrc_t out_rsrc(void* p, unsigned bytes) { return __builtin_amdgcn_make_buffer_rsrc(p, 0, bytes, 0x00020000); }
template <int AUX> __device__ __forceinline__ void bst16(rsrc_t r, unsigned voff, u32x4 v) { __builtin_amdgcn_raw_buffer_store_b128(v, r, voff, 0, AUX); }
template <int AUX> __device__ __forceinline__ void bst8(rsrc_t r, unsigned voff, unsigned soff, u32x2 v) { __builtin_amdgcn_raw_buffer_store_b64(v, r, voff, soff, AUX); }
#define WAIT_V(n) asm volatile("s_waitcnt vmcnt(" #n ")" ::: "memory")
#define WAIT_L0() __builtin_amdgcn_s_waitcnt(0xC07F)
#define OPAQUE(x) asm volatile("" : "+v"(x))
#define BAR() __builtin_amdgcn_s_barrier()
#define SCHED() __builtin_amdgcn_sched_barrier(0)

__device__ __forceinline__ u32x4 make_rsrc(const void* p, unsigned bytes) {
  uint64_t a = (uint64_t)p;
  u32x4 r;
  r.x = __builtin_amdgcn_readfirstlane((unsigned)a);
  r.y = __builtin_amdgcn_readfirstlane((unsigned)(a >> 32) & 0xffffu);
  r.z = bytes;
  r.w = 0x00020000u;
  return r;
}

__device__ __forceinline__ int swap23(int k) { return (k & ~12) | ((k & 4) << 1) | ((k & 8) >> 1); }

struct Stager {
  unsigned vx, vw[7], v4;
  u32x4 rx, rw, r4;
  unsigned sx, sw, s4;
  unsigned wstep, step4;
  unsigned lx, lw, l4;
};
__device__ __forceinline__ void stage_x(const Stager& st, unsigned sb) {
#pragma unroll
  for (int p = 0; p < 4; ++p) dma16(sb + st.lx + p * 4096, st.vx, st.rx, st.sx + p * (32 * D * 2));
}
__device__ __forceinline__ void stage_w(const Stager& st, unsigned sb) {
#pragma unroll
  for (int j = 0; j < 7; ++j) dma16(sb + st.lw + j * 4096, st.vw[j], st.rw, st.sw);
  dma16(sb + st.l4, st.v4, st.r4, st.s4);
}
__device__ __forceinline__ void stage_next(Stager& st) { st.sx += 128u; st.sw += st.wstep; st.s4 += st.step4; }
__device__ __forceinline__ unsigned w_src(int wi, int lane, unsigned rowbytes, unsigned secstride, int chmax) {
  unsigned ci = wi * 64 + lane;
  unsigned pos = (ci * 2185u) >> 16, ch = ci - pos * 30u;
  unsigned sec = (ch * 205u) >> 11;
  unsigned k = swap23(pos);
  ch = ch < (unsigned)chmax ? ch : (unsigned)chmax;
  return k * rowbytes + ch * 16u + sec * secstride;
}

struct AttnAcc { f32x4 q[8], k[9], v[9], e[9]; };
__device__ __forceinline__ u32x4 cat(u32x2 a, u32x2 b) { return u32x4{a.x, a.y, b.x, b.y}; }

template <int W>
struct AttnOps {
  struct Frags { u32x4 x[9]; u32x2 wl[4], wh[4]; };
  AttnAcc a;
  unsigned xa, wa, we;
  static constexpr int per_rb(int rb) { return 2 + (rb < 8 ? 1 : 0) + ((W == 3 || (W == 0 && rb == 8)) ? 0 : 1); }
  static constexpr int NM = per_rb(8) + 8 * per_rb(0);
  static constexpr int NR = W < 3 ? 17 : 15;
  template <int I> __device__ __forceinline__ void mma(const Frags& f) {
    constexpr int rb = I < per_rb(8) ? 8 : (I - per_rb(8)) / per_rb(0);
    constexpr int j = I < per_rb(8) ? I : (I - per_rb(8)) % per_rb(0);
    constexpr bool has_e = !(W == 3 || (W == 0 && rb == 8));
    if constexpr (j == 0) a.k[rb] = mfma16(cat(f.wl[1], f.wh[1]), f.x[rb], a.k[rb]);
    else if constexpr (j == 1) a.v[rb] = mfma16(f.x[rb], cat(f.wl[2], f.wh[2]), a.v[rb]);
    else if constexpr (j == 2 && has_e) {
      if constexpr (W == 2) a.e[rb] = mfma16(f.x[rb], cat(f.wl[3], f.wh[3]), a.e[rb]);
      else a.e[rb] = mfma16(cat(f.wl[3], f.wh[3]), f.x[rb], a.e[rb]);
    } else a.q[rb] = mfma16(cat(f.wl[0], f.wh[0]), f.x[rb], a.q[rb]);
  }
  unsigned x, w, e;
  __device__ __forceinline__ void bases(unsigned sb, int kk) {
    x = (xa ^ (kk * 64)) + sb; w = wa + sb + kk * 32 * W_ROW; e = we + sb + kk * 32 * W_ROW;
    OPAQUE(x); OPAQUE(w); if constexpr (W < 3) OPAQUE(e);
  }
  template <int I0> __device__ __forceinline__ void read(Frags& f) {
    constexpr int I = (W == 3 && I0 >= 5) ? I0 + 2 : I0;
    if constexpr (I == 0) f.wl[1] = lds_tr(w + 160);
    if constexpr (I == 1) f.wh[1] = lds_tr(w + 160 + 8 * W_ROW);
    if constexpr (I == 2) f.x[8] = lds_read128(x + 8 * 2048);
    if constexpr (I == 3) f.wl[2] = lds_tr(w + 320);
    if constexpr (I == 4) f.wh[2] = lds_tr(w + 320 + 8 * W_ROW);
    if constexpr (I == 5) f.wl[3] = lds_tr(e);
    if constexpr (I == 6) f.wh[3] = lds_tr(e + 8 * W_ROW);
    if constexpr (I == 7) f.x[0] = lds_read128(x);
    if constexpr (I == 8) f.wl[0] = lds_tr(w);
    if constexpr (I == 9) f.wh[0] = lds_tr(w + 8 * W_ROW);
    if constexpr (I >= 10) f.x[I - 9] = lds_read128(x + (I - 9) * 2048);
  }
};

struct MlpAcc { f32x4 c[4][7]; };
struct MlpOps {
  struct Frags { u32x4 x[4]; u32x2 wl[7], wh[7]; };
  MlpAcc a;
  unsigned xa, wa;
  static constexpr int NM = 28, NR = 18;
  template <int I> __device__ __forceinline__ void mma(const Frags& f) {
    constexpr int n = I / 4, r = I % 4;
    a.c[r][n] = mfma16(cat(f.wl[n], f.wh[n]), f.x[r], a.c[r][n]);
  }
  unsigned x, w, w6, d6;
  __device__ __forceinline__ void bases(unsigned sb, int kk) {
    x = (xa ^ (kk * 64)) + sb; w = wa + sb + kk * 32 * W_ROW; w6 = w + d6;
    OPAQUE(x); OPAQUE(w); OPAQUE(w6);
  }
  template <int I> __device__ __forceinline__ void read(Frags& f) {
    if constexpr (I < 2) { if constexpr (I == 0) f.wl[0] = lds_tr(w); else f.wh[0] = lds_tr(w + 8 * W_ROW); }
    else if constexpr (I < 6) f.x[I - 2] = lds_read128(x + (I - 2) * 2048);
    else {
      constexpr int n = 1 + (I - 6) / 2;
      const unsigned a = n < 6 ? w + 32 * n : w6;
      if constexpr ((I & 1) == 0) f.wl[n] = lds_tr(a); else f.wh[n] = lds_tr(a + 8 * W_ROW);
    }
  }
};

template <class OPS, int I>
__device__ __forceinline__ void read_all(OPS& o, typename OPS::Frags& f) {
  if constexpr (I < OPS::NR) { o.template read<I>(f); read_all<OPS, I + 1>(o, f); }
}
template <class OPS, int I>
__device__ __forceinline__ void half_step(OPS& o, const typename OPS::Frags& fc, typename OPS::Frags& fn) {
  if constexpr (I < OPS::NM) {
    o.template mma<I>(fc);
#define RD_TAIL 6
    constexpr int span = OPS::NM - RD_TAIL;
    static_assert(span >= OPS::NR, "one read per gap");
    constexpr int j = (I * OPS::NR + span - 1) / span;
    if constexpr (j < OPS::NR && j * span / OPS::NR == I) { SCHED(); o.template read<j>(fn); }
    SCHED();
    half_step<OPS, I + 1>(o, fc, fn);
  }
}

template <class OPS>
__device__ __forceinline__ void k_loop(OPS& o, int live) {
  typename OPS::Frags f0, f1;
  o.bases(0, 0);
  if (live) BAR();
  read_all<OPS, 0>(o, f0);
  unsigned sb = 0;
#pragma unroll 1
  for (int t = live ? 0 : NT1 - 1; t < NT1; ++t) {
    unsigned sn = sb + STAGE; if (sn == NSTAGE * STAGE) sn = 0;
    o.bases(sb, 1);
    WAIT_L0();
    SCHED();
    half_step<OPS, 0>(o, f0, f1);
    o.bases(sn, 0);
    WAIT_L0();
    if (t < NT1 - 1) BAR();
    SCHED();
    half_step<OPS, 0>(o, f1, f0);
    sb = sn;
  }
}

__device__ __forceinline__ float fmx(float a, float b) { return __builtin_elementwise_maximum(a, b); }
__device__ __forceinline__ void swap32(float& a, float& b) {
  auto r = __builtin_amdgcn_permlane32_swap(__builtin_bit_cast(unsigned, a), __builtin_bit_cast(unsigned, b), false, false);
  unsigned r0 = r[0], r1 = r[1];
  a = __builtin_bit_cast(float, r0); b = __builtin_bit_cast(float, r1);
}
__device__ __forceinline__ void swap16(float& a, float& b) {
  auto r = __builtin_amdgcn_permlane16_swap(__builtin_bit_cast(unsigned, a), __builtin_bit_cast(unsigned, b), false, false);
  unsigned r0 = r[0], r1 = r[1];
  a = __builtin_bit_cast(float, r0); b = __builtin_bit_cast(float, r1);
}
template <bool MAX>
__device__ __forceinline__ float reduce4(float x0, float x1, float x2, float x3) {
  swap32(x0, x1); swap32(x2, x3);
  float y01 = MAX ? fmx(x0, x1) : x0 + x1, y23 = MAX ? fmx(x2, x3) : x2 + x3;
  swap16(y01, y23);
  return MAX ? fmx(y01, y23) : y01 + y23;
}
__device__ __forceinline__ void bcast4(float z, float& x0, float& x1, float& x2, float& x3) {
  float u = z, w = z;
  swap16(u, w);
  x0 = u; x1 = u; swap32(x0, x1);
  x2 = w; x3 = w; swap32(x2, x3);
}

struct AttnPacked { u32x2 q[8], k[9], v[9]; };

template <int W>
__device__ __forceinline__ void attn_main(int lane, int live, AttnPacked& pk, unsigned char* __restrict__ ws) {
  OPAQUE(lane);
  const int c = lane & 15, g = lane >> 4, q4 = (lane >> 2) & 3, p4 = lane & 3;
  AttnOps<W> o;
#pragma unroll
  for (int i = 0; i < 9; ++i) {
    if (i < 8) o.a.q[i] = f32x4{0.f, 0.f, 0.f, 0.f};
    o.a.k[i] = f32x4{0.f, 0.f, 0.f, 0.f}; o.a.v[i] = f32x4{0.f, 0.f, 0.f, 0.f}; o.a.e[i] = f32x4{0.f, 0.f, 0.f, 0.f};
  }
  o.xa = c * 128 + ((g ^ ((c >> 1) & 7)) * 16);
  const unsigned wrow = X_BYTES + ((g >> 1) * 16 + (g & 1) * 4 + q4) * W_ROW;
  o.wa = wrow + 40 * W + 8 * p4;
  o.we = wrow + 160 * (W < 3 ? W : 0) + 40 * p4 + 32;
  STAMP(ws, 0, 1);
  k_loop(o, live);
  STAMP(ws, 0, 2);
  OPAQUE(lane);
  if constexpr (W < 3) {
    constexpr int base = W == 0 ? EX_Q : (W == 1 ? EX_K : EX_V);
#pragma unroll
    for (int i = 0; i < (W == 0 ? 8 : 9); ++i) {
      u32x2 v = {pack2(o.a.e[i][0], o.a.e[i][1]), pack2(o.a.e[i][2], o.a.e[i][3])};
      *(LAS u32x2*)(uintptr_t)(base + i * 512 + lane * 8) = v;
    }
  }
#pragma unroll
  for (int i = 0; i < 9; ++i) {
    if (i < 8) pk.q[i] = u32x2{pack2(o.a.q[i][0], o.a.q[i][1]), pack2(o.a.q[i][2], o.a.q[i][3])};
    pk.k[i] = u32x2{pack2(o.a.k[i][0], o.a.k[i][1]), pack2(o.a.k[i][2], o.a.k[i][3])};
    pk.v[i] = u32x2{pack2(o.a.v[i][0], o.a.v[i][1]), pack2(o.a.v[i][2], o.a.v[i][3])};
  }
}

__device__ __forceinline__ void attn_epilogue(int lane, int live, int W, int mt, int hg, const AttnPacked& pk, unsigned char* __restrict__ ws) {
  OPAQUE(lane);
  const int c = lane & 15, g = lane >> 4;
  f32x4 mprev, mcur, mfirst;
#pragma unroll
  for (int r = 0; r < 4; ++r) {
    int dcur = c - 4 * g - r, dprev = dcur + 16;
    mcur[r] = (dcur >= 0 && (dcur & 1) == 0) ? 0.f : -1e30f;
    mprev[r] = (dprev <= 14 && (dprev & 1) == 0) ? 0.f : -1e30f;
    mfirst[r] = mt == 0 ? -1e30f : mprev[r];
  }
  const float c2 = 0.22360679774997896f * 1.4426950408889634f;
  const unsigned sel_lo = (c & 1) ? 0x05040c0cu : 0x0c0c0504u, sel_hi = (c & 1) ? 0x07060c0cu : 0x0c0c0706u;
  unsigned exa = EX_OFF + W * 128 + c * 8 + g * 2, exv = EX_V + lane * 8;
  OPAQUE(exa); OPAQUE(exv);
  WAIT_L0();
  if (live) BAR();
  unsigned kx[9], qx[8]; u32x2 vx[9];
#pragma unroll
  for (int i = 0; i < 9; ++i) {
    kx[i] = *(const LAS unsigned short*)(uintptr_t)(exa + (EX_K - EX_OFF) + i * 512);
    if (i < 8) qx[i] = *(const LAS unsigned short*)(uintptr_t)(exa + (EX_Q - EX_OFF) + i * 512);
  }
#pragma unroll
  for (int i = 0; i < 9; ++i) vx[i] = lds_read64(exv + i * 512);
  float t[8][4], m[8];
  f32x4 sp_[8], sc_[8];
#pragma unroll
  for (int i = 0; i < 8; ++i) {
    const int ip = i == 0 ? 8 : i - 1;
    const u32x4 qf = {pk.q[i].x, pk.q[i].y, qx[i], 0u};
    sp_[i] = mfma16(u32x4{pk.k[ip].x, pk.k[ip].y, kx[ip], 0u}, qf, i == 0 ? mfirst : mprev);
    sc_[i] = mfma16(u32x4{pk.k[i].x, pk.k[i].y, kx[i], 0u}, qf, mcur);
  }
  __builtin_amdgcn_sched_group_barrier(0x008, 4, 0);
#pragma unroll
  for (int i = 0; i < 12; ++i) { __builtin_amdgcn_sched_group_barrier(0x008, 1, 0); __builtin_amdgcn_sched_group_barrier(0x002, 3, 0); }
#pragma unroll
  for (int i = 0; i < 8; ++i) {
    const f32x4 sp = sp_[i], sc = sc_[i];
    t[i][0] = fmx(sp[0], sp[1]); t[i][1] = fmx(sp[2], sp[3]); t[i][2] = fmx(sc[0], sc[1]); t[i][3] = fmx(sc[2], sc[3]);
    m[i] = fmx(fmx(t[i][0], t[i][1]), fmx(t[i][2], t[i][3]));
  }
  bcast4(reduce4<true>(m[0], m[1], m[2], m[3]) * -c2, m[0], m[1], m[2], m[3]);
  bcast4(reduce4<true>(m[4], m[5], m[6], m[7]) * -c2, m[4], m[5], m[6], m[7]);
  float l[8];
#pragma unroll
  for (int i = 0; i < 8; ++i) {
#pragma unroll
    for (int r = 0; r < 4; ++r) t[i][r] = __builtin_amdgcn_exp2f(fmaf(t[i][r], c2, m[i]));
    l[i] = (t[i][0] + t[i][1]) + (t[i][2] + t[i][3]);
  }
  bcast4(__builtin_amdgcn_rcpf(reduce4<false>(l[0], l[1], l[2], l[3])), l[0], l[1], l[2], l[3]);
  bcast4(__builtin_amdgcn_rcpf(reduce4<false>(l[4], l[5], l[6], l[7])), l[4], l[5], l[6], l[7]);
  const int H = hg * 4 + W;
  const rsrc_t ro = out_rsrc(ws + A2_OFF, live ? (unsigned)A2_BYTES : 0u);
  const unsigned am = (mt * 2 * KS2 + (H >> 1)) * 4096 + (H & 1) * 8;
  const unsigned ax = (mt * 2 * KS2 + 16 + (hg >> 1)) * 4096 + (hg & 1) * 8;
  const unsigned vo = lane * 16, vox = g == W ? vo : 0x40000000u;
#pragma unroll
  for (int i = 0; i < 8; ++i) {
    const int ip = i == 0 ? 8 : i - 1;
    const unsigned p01 = pack2(t[i][0] * l[i], t[i][1] * l[i]), p23 = pack2(t[i][2] * l[i], t[i][3] * l[i]);
    const u32x4 pf = {__builtin_amdgcn_perm(p01, 0u, sel_lo), __builtin_amdgcn_perm(p01, 0u, sel_hi),
                      __builtin_amdgcn_perm(p23, 0u, sel_lo), __builtin_amdgcn_perm(p23, 0u, sel_hi)};
    const f32x4 z = {0.f, 0.f, 0.f, 0.f};
    f32x4 om = mfma16(cat(pk.v[ip], pk.v[i]), pf, z);
    f32x4 ox = mfma16(cat(vx[ip], vx[i]), pf, z);
    bst8<0>(ro, vo + (i & 3) * 1024, am + (i >> 2) * (KS2 * 4096), u32x2{pack2(om[0], om[1]), pack2(om[2], om[3])});
    bst8<0>(ro, vox + (i & 3) * 1024, ax + (i >> 2) * (KS2 * 4096), u32x2{pack2(ox[0], ox[1]), pack2(ox[2], ox[3])});
  }
}

__device__ __forceinline__ unsigned mish2(float h0, float h1) {
  f32x2 h = {h0, h1};
  f32x2 a = h * 1.4426950408889634f;
  f32x2 w = {__builtin_amdgcn_exp2f(a.x), __builtin_amdgcn_exp2f(a.y)};
  w = w + 1.f;
  f32x2 d = __builtin_elementwise_fma(w, w, f32x2{1.f, 1.f});
  f32x2 r = {__builtin_amdgcn_rcpf(d.x), __builtin_amdgcn_rcpf(d.y)};
  f32x2 t = __builtin_elementwise_fma(r, f32x2{-2.f, -2.f}, f32x2{1.f, 1.f});
  f32x2 y = h * t;
  return pack2(y.x, y.y);
}

__device__ __forceinline__ void mlp_role(int lane, int live, int wave, int mt, int nb0, const u32x2 (&bias)[7],
                                         unsigned char* __restrict__ ws) {
  const int c = lane & 15, g = lane >> 4, q4 = (lane >> 2) & 3, p4 = lane & 3;
  const int wm = wave >> 1, wn = wave & 1;
  MlpOps o;
#pragma unroll
  for (int n = 0; n < 7; ++n) {
    f32x4 b = {bf_lo(bias[n].x), bf_hi(bias[n].x), bf_lo(bias[n].y), bf_hi(bias[n].y)};
#pragma unroll
    for (int r = 0; r < 4; ++r) o.a.c[r][n] = b;
  }
  o.xa = wm * 4 * 2048 + c * 128 + ((g ^ ((c >> 1) & 7)) * 16);
  o.wa = X_BYTES + ((g >> 1) * 16 + (g & 1) * 4 + q4) * W_ROW + wn * 8 * 32 + 8 * p4;
  o.d6 = wn ? (unsigned)-32 : 192u;
  STAMP(ws, 0, 1);
  k_loop(o, live);
  STAMP(ws, 0, 2);

  const rsrc_t ro = out_rsrc(ws + A2_OFF, live ? (unsigned)A2_BYTES : 0u);
  const unsigned so = ((mt * 2 + wm) * KS2 + 20 + (nb0 >> 1)) * 4096;
  const unsigned vo = lane * 16;
  unsigned vj[3];
#pragma unroll
  for (int j = 0; j < 3; ++j) { vj[j] = vo + so + (wn * 4 + j) * 4096; OPAQUE(vj[j]); }
#pragma unroll
  for (int r = 0; r < 4; ++r) {
#pragma unroll
    for (int j = 0; j < 3; ++j) {
      const f32x4 h0 = o.a.c[r][2 * j], h1 = o.a.c[r][2 * j + 1];
      bst16<ST_AUX>(ro, vj[j] + r * 1024,
                    u32x4{mish2(h0[0], h0[1]), mish2(h0[2], h0[3]), mish2(h1[0], h1[1]), mish2(h1[2], h1[3])});
    }
    const f32x4 h = o.a.c[r][6];
    bst8<0>(ro, vo + r * 1024, so + 3 * 4096 + wn * 8, u32x2{mish2(h[0], h[1]), mish2(h[2], h[3])});
  }
}

__device__ __forceinline__ int w2cat_row(int beta, int rho) {
  if (beta < 32) return 20 * beta + rho;
  if (beta < 40) return 20 * (4 * (beta - 32) + (rho >> 2)) + 16 + (rho & 3);
  return 16 * (beta - 40) + rho;
}

__global__ __launch_bounds__(512, 2) void k1_kernel(const unsigned short* __restrict__ x, const unsigned short* __restrict__ wqkv,
                                                    const unsigned short* __restrict__ wo, const unsigned short* __restrict__ w1,
                                                    const unsigned short* __restrict__ b1, const unsigned short* __restrict__ w2,
                                                    const unsigned short* __restrict__ gate_w, unsigned char* __restrict__ ws) {
  extern __shared__ __attribute__((aligned(16))) unsigned char smem[];
  const int lane = threadIdx.x & 63;
  const int wave = __builtin_amdgcn_readfirstlane(threadIdx.x >> 6);
  const int b = blockIdx.x, xcd = b & 7, idx = b >> 3;
  const int mt = (xcd & 1) * 8 + (idx & 7);
  const int nsel = idx >> 3;
  const int nt = (xcd >> 1) * 2 + (nsel & 1);
  const bool attn = nsel < 2;
  const int m0 = mt * 128;
  const int nb0 = nt * 14 < 94 ? nt * 14 : 94;
  STAMP(ws, 0, 0);
  if (idx == 16 + CODE_PF && wave < 4) {
    uint64_t pc;
    asm volatile("s_getpc_b64 %0" : "=s"(pc));
    dma_touch(K1_PF, (unsigned)(wave * 64 + lane) * 128u, make_rsrc((const void*)(pc & ~(uint64_t)255), CODE_BYTES), 0);
  }

  if (__builtin_expect(wave >= 4, 1)) {
    const int lw = wave - 4;
    Stager st;
    st.rx = make_rsrc(x, S_TOK * D * 2);
    st.vx = (unsigned)((8 * lw + (lane >> 3)) * (D * 2) + (((lane & 7) ^ ((4 * lw + (lane >> 4)) & 7)) * 16));
    st.sx = (unsigned)(m0 * D * 2);
    st.lx = lw * 1024;
    stage_x(st, 0);
    st.rw = make_rsrc(attn ? wqkv : w1, attn ? D * 3 * D * 2 : D * F * 2);
    const unsigned rowbytes = attn ? 3 * D * 2 : F * 2, secstride = attn ? D * 2 - 160 : 0;
    const int chmax = attn ? 29 : 27;
    st.wstep = 64u * rowbytes;
    st.sw = attn ? nt * 160 : nb0 * 32;
    st.lw = X_BYTES + lw * 1024;
#pragma unroll
    for (int j = 0; j < 7; ++j) st.vw[j] = w_src(lw + 4 * j, lane, rowbytes, secstride, chmax);
    {
      unsigned v4w = w_src(26 + lw, lane, rowbytes, secstride, chmax);
      const bool hx = lw < 2;
      st.v4 = hx ? st.vx : v4w;
      st.r4 = hx ? st.rx : st.rw;
      st.s4 = hx ? (m0 ? st.sx - 16 * D * 2 : 0u) : st.sw;
      st.step4 = hx ? 128u : st.wstep;
      st.l4 = hx ? (16 + lw) * 1024 : X_BYTES + (26 + lw) * 1024;
    }
    stage_w(st, 0); stage_next(st);
    stage_x(st, STAGE); stage_w(st, STAGE); stage_next(st);
    WAIT_V(12);
    STAMP(ws, 0, 1);
    BAR();
    stage_x(st, 2 * STAGE); stage_w(st, 2 * STAGE); stage_next(st);
    unsigned sb = 0;
#pragma unroll 1
    for (int t = 0; t < NT1 - 3; ++t) {
      WAIT_V(12);
      BAR();
      stage_x(st, sb); stage_w(st, sb); stage_next(st);
      sb += STAGE; if (sb == NSTAGE * STAGE) sb = 0;
    }
    WAIT_V(12); BAR();
    const int unit = lw * 256 + b;
    const bool tile = unit < KS2 * 8;
    const int ks = unit >> 3, ct = unit & 7;
    const unsigned lt = STAGE + lw * 5120;
    {
      const u32x4 r2 = make_rsrc(ks < 20 ? wo : w2, tile ? (ks < 20 ? D * D * 2 : F * D * 2) : 0);
#pragma unroll
      for (int i = 0; i < 5; ++i) {
        unsigned ci = i * 64 + lane, row = (ci * 205u) >> 11, ch = ci - row * 10u;
        dma16(lt + i * 1024, (unsigned)w2cat_row(2 * ks + (row >> 4), row & 15) * (D * 2) + ch * 16, r2, ct * 160);
      }
    }
    WAIT_V(5); BAR();
    const int tok = m0 + ((attn ? 0 : 8) + nt) * 8 + lw * 2 + (lane >> 5);
    {
      const u32x2* xr = (const u32x2*)(x + (size_t)tok * D) + (lane & 31);
      const u32x2* gp = (const u32x2*)gate_w + (lane & 31);
      float sg = 0.f;
#pragma unroll
      for (int j = 0; j < 5; ++j) {
        u32x2 a = xr[32 * j], w = gp[32 * j];
        sg = fmaf(bf_lo(a.x), bf_lo(w.x), sg); sg = fmaf(bf_hi(a.x), bf_hi(w.x), sg);
        sg = fmaf(bf_lo(a.y), bf_lo(w.y), sg); sg = fmaf(bf_hi(a.y), bf_hi(w.y), sg);
      }
#pragma unroll
      for (int o = 16; o >= 1; o >>= 1) sg += __shfl_xor(sg, o, 64);
      if ((lane & 31) == 0) ((float*)(ws + G_OFF))[tok] = 1.f / (1.f + __expf(-sg));
    }
    WAIT_V(1);
    if (tile) {
      const unsigned ra = lt + (4 * (lane >> 4) + ((lane >> 2) & 3)) * 160 + (lane & 3) * 40;
      unsigned char* bp = ws + B2_OFF + ((size_t)ct * KS2 + ks) * 5120 + lane * 16;
#pragma unroll
      for (int i = 0; i < 5; ++i) st16(bp + i * 1024, cat(lds_tr(ra + 8 * i), lds_tr(ra + 8 * i + 16 * 160)));
    }
    if (attn) BAR();
    STAMP(ws, 0, 3);
    return;
  }
  if (!attn) {
    const int pw = (b - 128) * 4 + wave;
    const int li = pw * 28 + (lane < 28 ? lane : 0);
    dma_touch(K1_PF, (unsigned)((li / 7) * (D * 2) + (3 + li % 7) * 128), make_rsrc(x, S_TOK * D * 2), 0);
    {
      const int q = ((idx - 16) * 4 + wave) * 63 + (lane < 63 ? lane : 0), row = q / 9, sl = q - row * 9;
      dma_touch(K1_PF, (unsigned)(row * (3 * D * 2) + (sl / 3) * (D * 2) + (sl % 3) * 128 + (xcd >> 1) * 320), make_rsrc(wqkv, D * 3 * D * 2), 192 * 3 * D * 2);
    }
    dma_touch(K1_PF, (unsigned)((pw * 24 + (lane < 24 ? lane : 0)) * 128), make_rsrc(w1, D * F * 2), 192 * F * 2);
  }
#define PASS0 1
  if (attn) {
#pragma nounroll
    for (int live = PASS0; live < 2; ++live) {
      asm volatile("" : "+s"(live));
      AttnPacked pk;
      if (wave == 0) attn_main<0>(lane, live, pk, ws);
      else if (wave == 1) attn_main<1>(lane, live, pk, ws);
      else if (wave == 2) attn_main<2>(lane, live, pk, ws);
      else attn_main<3>(lane, live, pk, ws);
      attn_epilogue(lane, live, wave, mt, nt, pk, ws);
    }
  } else {
    u32x2 bias[7];
    const u32x2* bp = (const u32x2*)(b1 + nb0 * 16 + (lane >> 4) * 4);
#pragma unroll
    for (int n = 0; n < 7; ++n) bias[n] = bp[(n < 6 ? (wave & 1) * 8 + n : 6 + (wave & 1)) * 4];
#pragma nounroll
    for (int live = PASS0; live < 2; ++live) {
      asm volatile("" : "+s"(live));
      mlp_role(lane, live, wave, mt, nb0, bias, ws);
    }
  }
  STAMP(ws, 0, 3);
}

constexpr int K2_STG = 36 * 1024, K2_NSLOT = 4, K2_NST = 20;
constexpr int K2_RING = K2_STG * K2_NSLOT, K2_LDS = K2_RING + 256;
#define K2_PFD 4

struct K2Frags { u32x4 a[2][2], b[2][5]; };
template <int I>
__device__ __forceinline__ void k2_step(f32x4 (&acc)[2][5], const K2Frags& fc, K2Frags& fn, unsigned ra, unsigned rb) {
  if constexpr (I < 20) {
    constexpr int k = I / 10, i = (I % 10) / 5, j = I % 5;
    acc[i][j] = mfma16(fc.b[k][j], fc.a[k][i], acc[i][j]);
    SCHED();
    if constexpr (I < 14) {
      constexpr int kk = I / 7, r = I % 7;
      if constexpr (r < 2) fn.a[kk][r] = lds_read128(ra + r * 1024 + kk * 9216);
      else fn.b[kk][r - 2] = lds_read128(rb + (r - 2) * 1024 + kk * 9216);
      SCHED();
    }
    k2_step<I + 1>(acc, fc, fn, ra, rb);
  }
}

__global__ __launch_bounds__(512, 2) void k2_kernel(const unsigned char* __restrict__ ws, unsigned short* __restrict__ out) {
  extern __shared__ __attribute__((aligned(16))) unsigned char smem[];
  const int lane = threadIdx.x & 63;
  const int wave = __builtin_amdgcn_readfirstlane(threadIdx.x >> 6);
  const int b = blockIdx.x, xcd = b & 7, idx = b >> 3;
  const int mt = (xcd & 3) * 8 + (idx & 7);
  const int nt = (xcd >> 2) * 4 + (idx >> 3);
  STAMP(ws, 1, 0);
  if (__builtin_expect(wave >= 4, 1)) {
    const int lw = wave - 4;
    const u32x4 rs = make_rsrc(ws, (unsigned)(A2_BYTES + B2_BYTES));
    const unsigned v = lane * 16;
    unsigned sa = (unsigned)A2_OFF + (mt * KS2 + lw) * 4096, sbb = (unsigned)B2_OFF + (nt * KS2 + lw) * 5120;
    unsigned l = lw * 9 * 1024;
    auto issue = [&](bool live) {
      const unsigned oob = live ? 0u : 0x40000000u;
#pragma unroll
      for (int i = 0; i < 4; ++i) dma16(l + i * 1024, v, rs, sa + i * 1024 + oob);
#pragma unroll
      for (int j = 0; j < 5; ++j) dma16(l + (4 + j) * 1024, v, rs, sbb + j * 1024 + oob);
      sa += 4 * 4096; sbb += 4 * 5120; l += K2_STG; if (l >= K2_RING) l -= K2_RING;
    };
    issue(true); issue(true); issue(true);
#pragma unroll 1
    for (int s = 0; s < K2_NST - 3; ++s) {
      WAIT_V(18);
      BAR();
      issue(4 * (s + 3) + lw < KS2);
    }
    WAIT_V(18); BAR();
    WAIT_V(9); BAR();
    WAIT_V(0); BAR();
    return;
  }
  const int c = lane & 15, g = lane >> 4;
  const int kh = wave >> 1, tp = wave & 1;
  const float* gp = (const float*)(ws + G_OFF) + mt * 64 + tp * 32 + c;
  const float gate0 = gp[0], gate1 = gp[16];
  f32x4 acc[2][5], res[2][5];
  K2Frags f0, f1;
#pragma unroll
  for (int j = 0; j < 5; ++j) {
    acc[0][j] = acc[1][j] = f32x4{0.f, 0.f, 0.f, 0.f};
    f1.b[0][j] = f1.b[1][j] = u32x4{0u, 0u, 0u, 0u};
  }
  f1.a[0][0] = f1.a[0][1] = f1.a[1][0] = f1.a[1][1] = u32x4{0u, 0u, 0u, 0u};
  const unsigned r0 = kh * 2 * 9216 + lane * 16;
  unsigned sl = 0;
  const u32x4 rs = make_rsrc(ws, (unsigned)(A2_BYTES + B2_BYTES));
  const unsigned pstep = tp ? 8 * 5120 : 8 * 4096;
  const unsigned pend = tp ? (unsigned)B2_OFF + (nt + 1) * KS2 * 5120 : (unsigned)A2_OFF + (mt + 1) * KS2 * 4096;
  unsigned pv = tp ? (unsigned)B2_OFF + nt * KS2 * 5120 + (idx & 7) * 5120 + (lane < 40 ? lane : 0) * 128
                   : (unsigned)A2_OFF + mt * KS2 * 4096 + (idx >> 3) * 8192 + lane * 128;
#pragma unroll 1
  for (int it = 0; it < K2_NST / 2; ++it) {
    unsigned ra = sl + r0 + tp * 2048, rb = sl + r0 + 4096;
    OPAQUE(ra); OPAQUE(rb);
    if (it == 3) {
#pragma unroll
      for (int j = 0; j < 5; ++j) {
        res[0][j] = acc[0][j] * gate0; res[1][j] = acc[1][j] * gate1;
        acc[0][j] = acc[1][j] = f32x4{0.f, 0.f, 0.f, 0.f};
      }
    }
    BAR();
    SCHED();
    k2_step<0>(acc, f1, f0, ra, rb);
    WAIT_L0();
    BAR();
    SCHED();
    k2_step<0>(acc, f0, f1, ra + K2_STG, rb + K2_STG);
    WAIT_L0();
    sl ^= 2 * K2_STG;
  }
  STAMP(ws, 1, 1);
  f32x4 keep[5];
#pragma unroll
  for (int j = 0; j < 5; ++j) {
    f32x4 p0 = res[0][j] + acc[0][j] * (1.f - gate0), p1 = res[1][j] + acc[1][j] * (1.f - gate1);
    keep[j] = kh ? p1 : p0;
    *(LAS f32x4*)(uintptr_t)((wave * 5 + j) * 1024 + lane * 16) = kh ? p0 : p1;
  }
  WAIT_L0();
  BAR();
  unsigned* op = (unsigned*)(out + (size_t)(mt * 64 + (2 * tp + kh) * 16 + c) * D + nt * 80 + g * 20);
  unsigned o[10];
#pragma unroll
  for (int j = 0; j < 5; ++j) {
    f32x4 r = keep[j] + *(const LAS f32x4*)(uintptr_t)(((wave ^ 2) * 5 + j) * 1024 + lane * 16);
    o[2 * j] = pack2(r[0], r[1]); o[2 * j + 1] = pack2(r[2], r[3]);
  }
  typedef u32x4 __attribute__((aligned(8))) u32x4_a8;
  *(u32x4_a8*)op = u32x4{o[0], o[1], o[2], o[3]};
  *(u32x4_a8*)(op + 4) = u32x4{o[4], o[5], o[6], o[7]};
  *(u32x2*)(op + 8) = u32x2{o[8], o[9]};
  STAMP(ws, 1, 2);
}

}

extern "C" void kernel_launch(void* const* d_in, const int* in_sizes, int n_in, void* d_out, int out_size, void* d_ws,
                              size_t ws_size, hipStream_t stream) {
  const unsigned short* x = (const unsigned short*)d_in[0];
  const unsigned short* wqkv = (const unsigned short*)d_in[1];
  const unsigned short* wo = (const unsigned short*)d_in[2];
  const unsigned short* w1 = (const unsigned short*)d_in[4];
  const unsigned short* b1 = (const unsigned short*)d_in[5];
  const unsigned short* w2 = (const unsigned short*)d_in[6];
  const unsigned short* gate_w = (const unsigned short*)d_in[7];
  (void)hipFuncSetAttribute((const void*)k1_kernel, hipFuncAttributeMaxDynamicSharedMemorySize, K1_LDS);
  (void)hipFuncSetAttribute((const void*)k2_kernel, hipFuncAttributeMaxDynamicSharedMemorySize, K2_LDS);
  k1_kernel<<<256, 512, K1_LDS, stream>>>(x, wqkv, wo, w1, b1, w2, gate_w, (unsigned char*)d_ws);
  k2_kernel<<<256, 512, K2_LDS, stream>>>((const unsigned char*)d_ws, (unsigned short*)d_out);
}
```

```cpp
#include <hip/hip_runtime.h>
#include <hip/hip_bf16.h>
#include <stdint.h>

#define STAMP(ws, kern, i)
#define CODE_BYTES 39424

namespace {

typedef __attribute__((ext_vector_type(4))) short s16x4;
typedef __attribute__((ext_vector_type(8))) __bf16 bf16x8;
typedef __attribute__((ext_vector_type(2))) __bf16 bf16x2;
typedef __attribute__((ext_vector_type(4))) float f32x4;
typedef __attribute__((ext_vector_type(2))) float f32x2;
typedef __attribute__((ext_vector_type(4))) unsigned u32x4;
typedef __attribute__((ext_vector_type(2))) unsigned u32x2;
#define LAS __attribute__((address_space(3)))

constexpr int S_TOK = 2048, D = 640, F = 1728;
constexpr int KS2 = 74;
constexpr size_t A2_BYTES = (size_t)128 * KS2 * 1024;
constexpr size_t B2_BYTES = (size_t)40 * KS2 * 1024;
constexpr size_t A2_OFF = 0, B2_OFF = A2_OFF + A2_BYTES, G_OFF = B2_OFF + B2_BYTES;
constexpr size_t SY_OFF = G_OFF + 8192;
constexpr int SY_BASE = 0, SY_PASS = 256, SY_FB = 512, SY_FA = 1024, SY_FE = 2048;

constexpr int X_BYTES = 144 * 128;
constexpr int W_ROW = 480;
constexpr int W_BYTES = 64 * W_ROW;
constexpr int STAGE = X_BYTES + W_BYTES;
constexpr int NSTAGE = 3;
constexpr int EX_OFF = NSTAGE * STAGE;
constexpr int EX_Q = EX_OFF, EX_K = EX_Q + 8 * 512, EX_V = EX_K + 9 * 512;
constexpr int K1_PF = EX_OFF + 3 * 5120;
constexpr int K1_BASES = EX_V + 9 * 512;
static_assert(K1_BASES + 2048 <= K1_PF, "LDS map");
constexpr int K1_NEWS = K1_PF + 256;
constexpr int K1_G = K1_NEWS + 64;
constexpr int K1_LDS = K1_G + 512;
constexpr int NT1 = 10;

__device__ __forceinline__ unsigned pack2(float a, float b) {
  f32x2 v = {a, b};
  bf16x2 r = __builtin_convertvector(v, bf16x2);
  return __builtin_bit_cast(unsigned, r);
}
__device__ __forceinline__ float bf_lo(unsigned u) { return __builtin_bit_cast(float, u << 16); }
__device__ __forceinline__ float bf_hi(unsigned u) { return __builtin_bit_cast(float, u & 0xffff0000u); }
__device__ __forceinline__ f32x4 mfma16(u32x4 a, u32x4 b, f32x4 c) {
  return __builtin_amdgcn_mfma_f32_16x16x32_bf16(__builtin_bit_cast(bf16x8, a), __builtin_bit_cast(bf16x8, b), c, 0, 0, 0);
}
__device__ __forceinline__ u32x4 lds_read128(unsigned addr) { return *(const LAS u32x4*)(uintptr_t)addr; }
__device__ __forceinline__ u32x2 lds_read64(unsigned addr) { return *(const LAS u32x2*)(uintptr_t)addr; }
__device__ __forceinline__ u32x2 lds_tr(unsigned addr) {
  s16x4 v = __builtin_amdgcn_ds_read_tr16_b64_v4i16((LAS s16x4*)(uintptr_t)addr);
  return __builtin_bit_cast(u32x2, v);
}
__device__ __forceinline__ u32x4 lds_wfrag(unsigned addr) {
  u32x2 a = lds_tr(addr), b = lds_tr(addr + 8 * W_ROW);
  u32x4 r = {a.x, a.y, b.x, b.y};
  return r;
}
__device__ __forceinline__ void dma16(unsigned ldsaddr, unsigned voff, u32x4 rsrc, unsigned soff) {
  asm volatile("s_mov_b32 m0, %0\n\ts_nop 0\n\tbuffer_load_dwordx4 %1, %2, %3 offen lds"
               :: "s"(ldsaddr), "v"(voff), "s"(rsrc), "s"(soff) : "memory");
}
__device__ __forceinline__ void dma16_dev(unsigned ldsaddr, unsigned voff, u32x4 rsrc, unsigned soff) {
  asm volatile("s_mov_b32 m0, %0\n\ts_nop 0\n\tbuffer_load_dwordx4 %1, %2, %3 offen sc1 lds"
               :: "s"(ldsaddr), "v"(voff), "s"(rsrc), "s"(soff) : "memory");
}
#define PF_FLAGS ""
__device__ __forceinline__ void dma_touch(unsigned ldsaddr, unsigned voff, u32x4 rsrc, unsigned soff) {
  asm volatile("s_mov_b32 m0, %0\n\ts_nop 0\n\tbuffer_load_dword %1, %2, %3 offen " PF_FLAGS " lds"
               :: "s"(ldsaddr), "v"(voff), "s"(rsrc), "s"(soff) : "memory");
}
#define ST_FLAGS "sc1"
__device__ __forceinline__ void st16(void* p, u32x4 v) {
  asm volatile("global_store_dwordx4 %0, %1, off " ST_FLAGS "\n\ts_nop 1" :: "v"(p), "v"(v) : "memory");
}
__device__ __forceinline__ void st8(void* p, u32x2 v) {
  asm volatile("global_store_dwordx2 %0, %1, off " ST_FLAGS :: "v"(p), "v"(v) : "memory");
}
#define ST_AUX 16
typedef __amdgpu_buffer_rsrc_t rsrc_t;
__device__ __forceinline__ rsrc_t out_rsrc(void* p, unsigned bytes) { return __builtin_amdgcn_make_buffer_rsrc(p, 0, bytes, 0x00020000); }
template <int AUX> __device__ __forceinline__ void bst16(rsrc_t r, unsigned voff, u32x4 v) { __builtin_amdgcn_raw_buffer_store_b128(v, r, voff, 0, AUX); }
template <int AUX> __device__ __forceinline__ void bst8(rsrc_t r, unsigned voff, unsigned soff, u32x2 v) { __builtin_amdgcn_raw_buffer_store_b64(v, r, voff, soff, AUX); }
#define WAIT_V(n) asm volatile("s_waitcnt vmcnt(" #n ")" ::: "memory")
#define WAIT_L0() __builtin_amdgcn_s_waitcnt(0xC07F)
#define OPAQUE(x) asm volatile("" : "+v"(x))
#define BAR() __builtin_amdgcn_s_barrier()
#define SCHED() __builtin_amdgcn_sched_barrier(0)

__device__ __forceinline__ u32x4 make_rsrc(const void* p, unsigned bytes) {
  uint64_t a = (uint64_t)p;
  u32x4 r;
  r.x = __builtin_amdgcn_readfirstlane((unsigned)a);
  r.y = __builtin_amdgcn_readfirstlane((unsigned)(a >> 32) & 0xffffu);
  r.z = bytes;
  r.w = 0x00020000u;
  return r;
}

__device__ __forceinline__ int swap23(int k) { return (k & ~12) | ((k & 4) << 1) | ((k & 8) >> 1); }

struct Stager {
  unsigned vx, vw[7], v4;
  u32x4 rx, rw, r4;
  unsigned sx, sw, s4;
  unsigned wstep, step4;
  unsigned lx, lw, l4;
};
__device__ __forceinline__ void stage_x(const Stager& st, unsigned sb) {
#pragma unroll
  for (int p = 0; p < 4; ++p) dma16(sb + st.lx + p * 4096, st.vx, st.rx, st.sx + p * (32 * D * 2));
}
__device__ __forceinline__ void stage_w(const Stager& st, unsigned sb) {
#pragma unroll
  for (int j = 0; j < 7; ++j) dma16(sb + st.lw + j * 4096, st.vw[j], st.rw, st.sw);
  dma16(sb + st.l4, st.v4, st.r4, st.s4);
}
__device__ __forceinline__ void stage_next(Stager& st) { st.sx += 128u; st.sw += st.wstep; st.s4 += st.step4; }
__device__ __forceinline__ unsigned w_src(int wi, int lane, unsigned rowbytes, unsigned secstride, int chmax) {
  unsigned ci = wi * 64 + lane;
  unsigned pos = (ci * 2185u) >> 16, ch = ci - pos * 30u;
  unsigned sec = (ch * 205u) >> 11;
  unsigned k = swap23(pos);
  ch = ch < (unsigned)chmax ? ch : (unsigned)chmax;
  return k * rowbytes + ch * 16u + sec * secstride;
}

struct AttnAcc { f32x4 q[8], k[9], v[9], e[9]; };
__device__ __forceinline__ u32x4 cat(u32x2 a, u32x2 b) { return u32x4{a.x, a.y, b.x, b.y}; }

template <int W>
struct AttnOps {
  struct Frags { u32x4 x[9]; u32x2 wl[4], wh[4]; };
  AttnAcc a;
  unsigned xa, wa, we;
  static constexpr int per_rb(int rb) { return 2 + (rb < 8 ? 1 : 0) + ((W == 3 || (W == 0 && rb == 8)) ? 0 : 1); }
  static constexpr int NM = per_rb(8) + 8 * per_rb(0);
  static constexpr int NR = W < 3 ? 17 : 15;
  template <int I> __device__ __forceinline__ void mma(const Frags& f) {
    constexpr int rb = I < per_rb(8) ? 8 : (I - per_rb(8)) / per_rb(0);
    constexpr int j = I < per_rb(8) ? I : (I - per_rb(8)) % per_rb(0);
    constexpr bool has_e = !(W == 3 || (W == 0 && rb == 8));
    if constexpr (j == 0) a.k[rb] = mfma16(cat(f.wl[1], f.wh[1]), f.x[rb], a.k[rb]);
    else if constexpr (j == 1) a.v[rb] = mfma16(f.x[rb], cat(f.wl[2], f.wh[2]), a.v[rb]);
    else if constexpr (j == 2 && has_e) {
      if constexpr (W == 2) a.e[rb] = mfma16(f.x[rb], cat(f.wl[3], f.wh[3]), a.e[rb]);
      else a.e[rb] = mfma16(cat(f.wl[3], f.wh[3]), f.x[rb], a.e[rb]);
    } else a.q[rb] = mfma16(cat(f.wl[0], f.wh[0]), f.x[rb], a.q[rb]);
  }
  unsigned x, w, e;
  __device__ __forceinline__ void bases(unsigned sb, int kk) {
    x = (xa ^ (kk * 64)) + sb; w = wa + sb + kk * 32 * W_ROW; e = we + sb + kk * 32 * W_ROW;
    OPAQUE(x); OPAQUE(w); if constexpr (W < 3) OPAQUE(e);
  }
  template <int I0> __device__ __forceinline__ void read(Frags& f) {
    constexpr int I = (W == 3 && I0 >= 5) ? I0 + 2 : I0;
    if constexpr (I == 0) f.wl[1] = lds_tr(w + 160);
    if constexpr (I == 1) f.wh[1] = lds_tr(w + 160 + 8 * W_ROW);
    if constexpr (I == 2) f.x[8] = lds_read128(x + 8 * 2048);
    if constexpr (I == 3) f.wl[2] = lds_tr(w + 320);
    if constexpr (I == 4) f.wh[2] = lds_tr(w + 320 + 8 * W_ROW);
    if constexpr (I == 5) f.wl[3] = lds_tr(e);
    if constexpr (I == 6) f.wh[3] = lds_tr(e + 8 * W_ROW);
    if constexpr (I == 7) f.x[0] = lds_read128(x);
    if constexpr (I == 8) f.wl[0] = lds_tr(w);
    if constexpr (I == 9) f.wh[0] = lds_tr(w + 8 * W_ROW);
    if constexpr (I >= 10) f.x[I - 9] = lds_read128(x + (I - 9) * 2048);
  }
};

struct MlpAcc { f32x4 c[4][7]; };
struct MlpOps {
  struct Frags { u32x4 x[4]; u32x2 wl[7], wh[7]; };
  MlpAcc a;
  unsigned xa, wa;
  static constexpr int NM = 28, NR = 18;
  template <int I> __device__ __forceinline__ void mma(const Frags& f) {
    constexpr int n = I / 4, r = I % 4;
    a.c[r][n] = mfma16(cat(f.wl[n], f.wh[n]), f.x[r], a.c[r][n]);
  }
  unsigned x, w, w6, d6;
  __device__ __forceinline__ void bases(unsigned sb, int kk) {
    x = (xa ^ (kk * 64)) + sb; w = wa + sb + kk * 32 * W_ROW; w6 = w + d6;
    OPAQUE(x); OPAQUE(w); OPAQUE(w6);
  }
  template <int I> __device__ __forceinline__ void read(Frags& f) {
    if constexpr (I < 2) { if constexpr (I == 0) f.wl[0] = lds_tr(w); else f.wh[0] = lds_tr(w + 8 * W_ROW); }
    else if constexpr (I < 6) f.x[I - 2] = lds_read128(x + (I - 2) * 2048);
    else {
      constexpr int n = 1 + (I - 6) / 2;
      const unsigned a = n < 6 ? w + 32 * n : w6;
      if constexpr ((I & 1) == 0) f.wl[n] = lds_tr(a); else f.wh[n] = lds_tr(a + 8 * W_ROW);
    }
  }
};

template <class OPS, int I>
__device__ __forceinline__ void read_all(OPS& o, typename OPS::Frags& f) {
  if constexpr (I < OPS::NR) { o.template read<I>(f); read_all<OPS, I + 1>(o, f); }
}
template <class OPS, int I>
__device__ __forceinline__ void half_step(OPS& o, const typename OPS::Frags& fc, typename OPS::Frags& fn) {
  if constexpr (I < OPS::NM) {
    o.template mma<I>(fc);
#define RD_TAIL 6
    constexpr int span = OPS::NM - RD_TAIL;
    static_assert(span >= OPS::NR, "one read per gap");
    constexpr int j = (I * OPS::NR + span - 1) / span;
    if constexpr (j < OPS::NR && j * span / OPS::NR == I) { SCHED(); o.template read<j>(fn); }
    SCHED();
    half_step<OPS, I + 1>(o, fc, fn);
  }
}

template <class OPS>
__device__ __forceinline__ void k_loop(OPS& o) {
  typename OPS::Frags f0, f1;
  o.bases(0, 0);
  BAR();
  read_all<OPS, 0>(o, f0);
  unsigned sb = 0;
#pragma unroll 1
  for (int t = 0; t < NT1; ++t) {
    unsigned sn = sb + STAGE; if (sn == NSTAGE * STAGE) sn = 0;
    o.bases(sb, 1);
    WAIT_L0();
    SCHED();
    half_step<OPS, 0>(o, f0, f1);
    o.bases(sn, 0);
    WAIT_L0();
    if (t == 2) WAIT_V(0);
    BAR();
    SCHED();
    half_step<OPS, 0>(o, f1, f0);
    sb = sn;
  }
}

__device__ __forceinline__ float fmx(float a, float b) { return __builtin_elementwise_maximum(a, b); }
__device__ __forceinline__ void swap32(float& a, float& b) {
  auto r = __builtin_amdgcn_permlane32_swap(__builtin_bit_cast(unsigned, a), __builtin_bit_cast(unsigned, b), false, false);
  unsigned r0 = r[0], r1 = r[1];
  a = __builtin_bit_cast(float, r0); b = __builtin_bit_cast(float, r1);
}
__device__ __forceinline__ void swap16(float& a, float& b) {
  auto r = __builtin_amdgcn_permlane16_swap(__builtin_bit_cast(unsigned, a), __builtin_bit_cast(unsigned, b), false, false);
  unsigned r0 = r[0], r1 = r[1];
  a = __builtin_bit_cast(float, r0); b = __builtin_bit_cast(float, r1);
}
__device__ __forceinline__ u32x4 widen_pair(u32x2 a, u32x2 b) {
  auto x = __builtin_amdgcn_permlane16_swap(a.x, b.x, false, false);
  auto y = __builtin_amdgcn_permlane16_swap(a.y, b.y, false, false);
  unsigned x0 = x[0], x1 = x[1], y0 = y[0], y1 = y[1];
  return u32x4{x0, y0, x1, y1};
}
template <bool MAX>
__device__ __forceinline__ float reduce4(float x0, float x1, float x2, float x3) {
  swap32(x0, x1); swap32(x2, x3);
  float y01 = MAX ? fmx(x0, x1) : x0 + x1, y23 = MAX ? fmx(x2, x3) : x2 + x3;
  swap16(y01, y23);
  return MAX ? fmx(y01, y23) : y01 + y23;
}
__device__ __forceinline__ void bcast4(float z, float& x0, float& x1, float& x2, float& x3) {
  float u = z, w = z;
  swap16(u, w);
  x0 = u; x1 = u; swap32(x0, x1);
  x2 = w; x3 = w; swap32(x2, x3);
}

constexpr int SPIN_CAP = 1 << 16;
__device__ __forceinline__ void lds_wait(int word, unsigned atleast) {
  volatile LAS unsigned* p = (volatile LAS unsigned*)(uintptr_t)(K1_NEWS + word * 4);
#pragma unroll 1
  for (int i = 0; i < 16 * SPIN_CAP; ++i) { if (__builtin_amdgcn_readfirstlane(*p) >= atleast) break; __builtin_amdgcn_s_sleep(1); }
}
__device__ __forceinline__ void lds_post(int word, unsigned v, int lane) {
  if (lane == 0) *(volatile LAS unsigned*)(uintptr_t)(K1_NEWS + word * 4) = v;
}

struct AttnPacked { u32x2 q[8], k[9], v[9]; };

template <int W>
__device__ __forceinline__ void attn_main(int lane, AttnPacked& pk, unsigned char* __restrict__ ws) {
  OPAQUE(lane);
  const int c = lane & 15, g = lane >> 4, q4 = (lane >> 2) & 3, p4 = lane & 3;
  AttnOps<W> o;
#pragma unroll
  for (int i = 0; i < 9; ++i) {
    if (i < 8) o.a.q[i] = f32x4{0.f, 0.f, 0.f, 0.f};
    o.a.k[i] = f32x4{0.f, 0.f, 0.f, 0.f}; o.a.v[i] = f32x4{0.f, 0.f, 0.f, 0.f}; o.a.e[i] = f32x4{0.f, 0.f, 0.f, 0.f};
  }
  o.xa = c * 128 + ((g ^ ((c >> 1) & 7)) * 16);
  const unsigned wrow = X_BYTES + ((g >> 1) * 16 + (g & 1) * 4 + q4) * W_ROW;
  o.wa = wrow + 40 * W + 8 * p4;
  o.we = wrow + 160 * (W < 3 ? W : 0) + 40 * p4 + 32;
  STAMP(ws, 0, 1);
  k_loop(o);
  STAMP(ws, 0, 2);
  OPAQUE(lane);
  if constexpr (W < 3) {
    constexpr int base = W == 0 ? EX_Q : (W == 1 ? EX_K : EX_V);
#pragma unroll
    for (int i = 0; i < (W == 0 ? 8 : 9); ++i) {
      u32x2 v = {pack2(o.a.e[i][0], o.a.e[i][1]), pack2(o.a.e[i][2], o.a.e[i][3])};
      *(LAS u32x2*)(uintptr_t)(base + i * 512 + lane * 8) = v;
    }
  }
#pragma unroll
  for (int i = 0; i < 9; ++i) {
    if (i < 8) pk.q[i] = u32x2{pack2(o.a.q[i][0], o.a.q[i][1]), pack2(o.a.q[i][2], o.a.q[i][3])};
    pk.k[i] = u32x2{pack2(o.a.k[i][0], o.a.k[i][1]), pack2(o.a.k[i][2], o.a.k[i][3])};
    pk.v[i] = u32x2{pack2(o.a.v[i][0], o.a.v[i][1]), pack2(o.a.v[i][2], o.a.v[i][3])};
  }
}

__device__ __forceinline__ void attn_epilogue(int lane, int W, int mt, int hg, AttnPacked& pk, unsigned char* __restrict__ ws) {
  OPAQUE(lane);
  const int c = lane & 15, g = lane >> 4;
  f32x4 mprev, mcur, mfirst;
#pragma unroll
  for (int r = 0; r < 4; ++r) {
    int dcur = c - 4 * g - r, dprev = dcur + 16;
    mcur[r] = (dcur >= 0 && (dcur & 1) == 0) ? 0.f : -1e30f;
    mprev[r] = (dprev <= 14 && (dprev & 1) == 0) ? 0.f : -1e30f;
    mfirst[r] = mt == 0 ? -1e30f : mprev[r];
  }
  const float c2 = 0.22360679774997896f * 1.4426950408889634f;
  const unsigned sel_lo = (c & 1) ? 0x05040c0cu : 0x0c0c0504u, sel_hi = (c & 1) ? 0x07060c0cu : 0x0c0c0706u;
  unsigned exa = EX_OFF + W * 128 + c * 8 + g * 2, exv = EX_V + (g * 16 + W * 4 + (c & 3)) * 8;
  unsigned exg = K1_G + c * 4;
  OPAQUE(exa); OPAQUE(exv); OPAQUE(exg);
  WAIT_L0();
  if (W < 3 && lane == 0) __hip_atomic_fetch_add((LAS unsigned*)(uintptr_t)(K1_NEWS + 4), 1u, __ATOMIC_RELAXED, __HIP_MEMORY_SCOPE_WORKGROUP);
#pragma unroll
  for (int i = 0; i < 9; ++i) { if (i < 8) OPAQUE(pk.q[i]); OPAQUE(pk.k[i]); OPAQUE(pk.v[i]); }
  OPAQUE(mprev); OPAQUE(mcur); OPAQUE(mfirst);
  lds_wait(1, 3u);
  asm volatile("" ::: "memory");
  unsigned kx[9], qx[8]; u32x2 vx[9];
#pragma unroll
  for (int i = 0; i < 9; ++i) {
    kx[i] = *(const LAS unsigned short*)(uintptr_t)(exa + (EX_K - EX_OFF) + i * 512);
    if (i < 8) qx[i] = *(const LAS unsigned short*)(uintptr_t)(exa + (EX_Q - EX_OFF) + i * 512);
  }
#pragma unroll
  for (int i = 0; i < 9; ++i) vx[i] = lds_read64(exv + i * 512);
  float gt[8];
#pragma unroll
  for (int i = 0; i < 8; ++i) gt[i] = *(const LAS float*)(uintptr_t)(exg + i * 64);
  float t[8][4], m[8];
  f32x4 sp_[8], sc_[8];
#pragma unroll
  for (int i = 0; i < 8; ++i) {
    const int ip = i == 0 ? 8 : i - 1;
    const u32x4 qf = {pk.q[i].x, pk.q[i].y, qx[i], 0u};
    sp_[i] = mfma16(u32x4{pk.k[ip].x, pk.k[ip].y, kx[ip], 0u}, qf, i == 0 ? mfirst : mprev);
    sc_[i] = mfma16(u32x4{pk.k[i].x, pk.k[i].y, kx[i], 0u}, qf, mcur);
  }
  SCHED();
#pragma unroll
  for (int i = 0; i < 8; ++i) {
    const f32x4 sp = sp_[i], sc = sc_[i];
    t[i][0] = fmx(sp[0], sp[1]); t[i][1] = fmx(sp[2], sp[3]); t[i][2] = fmx(sc[0], sc[1]); t[i][3] = fmx(sc[2], sc[3]);
    m[i] = fmx(fmx(t[i][0], t[i][1]), fmx(t[i][2], t[i][3]));
  }
  bcast4(reduce4<true>(m[0], m[1], m[2], m[3]) * -c2, m[0], m[1], m[2], m[3]);
  bcast4(reduce4<true>(m[4], m[5], m[6], m[7]) * -c2, m[4], m[5], m[6], m[7]);
  float l[8];
#pragma unroll
  for (int i = 0; i < 8; ++i) {
#pragma unroll
    for (int r = 0; r < 4; ++r) t[i][r] = __builtin_amdgcn_exp2f(fmaf(t[i][r], c2, m[i]));
    l[i] = (t[i][0] + t[i][1]) + (t[i][2] + t[i][3]);
  }
  bcast4(__builtin_amdgcn_rcpf(reduce4<false>(l[0], l[1], l[2], l[3])), l[0], l[1], l[2], l[3]);
  bcast4(__builtin_amdgcn_rcpf(reduce4<false>(l[4], l[5], l[6], l[7])), l[4], l[5], l[6], l[7]);
  const int H = hg * 4 + W;
  const rsrc_t ro = out_rsrc(ws + A2_OFF, (unsigned)A2_BYTES);
  unsigned vm = (mt * 2 * KS2 + (H >> 1)) * 4096 + (H & 1) * 512 + (g & 1) * 1024 + (g >> 1) * 256 + c * 16;
  unsigned vx_ = (mt * 2 * KS2 + 16 + (hg >> 1)) * 4096 + (hg & 1) * 512 + g * 1024 + (W >> 1) * 256 + c * 16 + (W & 1) * 8;
  u32x4 pf[8];
#pragma unroll
  for (int i = 0; i < 8; ++i) {
    const float sc = l[i] * gt[i];
    const unsigned p01 = pack2(t[i][0] * sc, t[i][1] * sc), p23 = pack2(t[i][2] * sc, t[i][3] * sc);
    pf[i] = u32x4{__builtin_amdgcn_perm(p01, 0u, sel_lo), __builtin_amdgcn_perm(p01, 0u, sel_hi),
                  __builtin_amdgcn_perm(p23, 0u, sel_lo), __builtin_amdgcn_perm(p23, 0u, sel_hi)};
  }
  f32x4 om[8], ox[8];
#pragma unroll
  for (int i = 0; i < 8; ++i) {
    const int ip = i == 0 ? 8 : i - 1;
    const f32x4 z = {0.f, 0.f, 0.f, 0.f};
    om[i] = mfma16(cat(pk.v[ip], pk.v[i]), pf[i], z);
    ox[i] = mfma16(cat(vx[ip], vx[i]), pf[i], z);
  }
  SCHED();
  const bool g1 = g & 1, g2 = g & 2;
#pragma unroll
  for (int h = 0; h < 2; ++h) {
    u32x2 sm[4];
    unsigned ex[4], ey[4];
#pragma unroll
    for (int i = 0; i < 4; ++i) {
      sm[i] = u32x2{pack2(om[4 * h + i][0], om[4 * h + i][1]), pack2(om[4 * h + i][2], om[4 * h + i][3])};
      ex[i] = pack2(ox[4 * h + i][0], ox[4 * h + i][1]); ey[i] = pack2(ox[4 * h + i][2], ox[4 * h + i][3]);
    }
    bst16<ST_AUX>(ro, vm, widen_pair(sm[0], sm[1]));
    bst16<ST_AUX>(ro, vm + 2048, widen_pair(sm[2], sm[3]));
    unsigned x01 = g1 ? ex[1] : ex[0], x23 = g1 ? ex[3] : ex[2], y01 = g1 ? ey[1] : ey[0], y23 = g1 ? ey[3] : ey[2];
    OPAQUE(x01); OPAQUE(x23); OPAQUE(y01); OPAQUE(y23);
    bst8<ST_AUX>(ro, vx_, 0, u32x2{g2 ? x23 : x01, g2 ? y23 : y01});
    vm += KS2 * 4096; vx_ += KS2 * 4096;
  }
}

__device__ __forceinline__ unsigned mish2(float h0, float h1, float sc) {
  f32x2 h = {h0, h1};
  f32x2 a = h * 1.4426950408889634f;
  f32x2 w = {__builtin_amdgcn_exp2f(a.x), __builtin_amdgcn_exp2f(a.y)};
  w = w + 1.f;
  f32x2 d = __builtin_elementwise_fma(w, w, f32x2{1.f, 1.f});
  f32x2 r = {__builtin_amdgcn_rcpf(d.x), __builtin_amdgcn_rcpf(d.y)};
  f32x2 t = __builtin_elementwise_fma(r, f32x2{-2.f * sc, -2.f * sc}, f32x2{sc, sc});
  f32x2 y = h * t;
  return pack2(y.x, y.y);
}

typedef unsigned long long u64;
typedef __attribute__((address_space(1))) u64 gu64;
__device__ __forceinline__ u64 sy_ld(const gu64* p) { return __hip_atomic_load(p, __ATOMIC_RELAXED, __HIP_MEMORY_SCOPE_AGENT); }
__device__ __forceinline__ void sy_st(gu64* p, u64 v) { __hip_atomic_store(p, v, __ATOMIC_RELAXED, __HIP_MEMORY_SCOPE_AGENT); }

__device__ __forceinline__ void mlp_role(int lane, int wave, int mt, int nb0, const u32x2 (&bias)[7],
                                         unsigned char* __restrict__ ws, gu64* early, u64 epoch) {
  const int c = lane & 15, g = lane >> 4, q4 = (lane >> 2) & 3, p4 = lane & 3;
  const int wm = wave >> 1, wn = wave & 1;
  MlpOps o;
#pragma unroll
  for (int n = 0; n < 7; ++n) {
    f32x4 b = {bf_lo(bias[n].x), bf_hi(bias[n].x), bf_lo(bias[n].y), bf_hi(bias[n].y)};
#pragma unroll
    for (int r = 0; r < 4; ++r) o.a.c[r][n] = b;
  }
  o.xa = wm * 4 * 2048 + c * 128 + ((g ^ ((c >> 1) & 7)) * 16);
  o.wa = X_BYTES + ((g >> 1) * 16 + (g & 1) * 4 + q4) * W_ROW + wn * 8 * 32 + 8 * p4;
  o.d6 = wn ? (unsigned)-32 : 192u;
  STAMP(ws, 0, 1);
  k_loop(o);
  STAMP(ws, 0, 2);

  const rsrc_t ro = out_rsrc(ws + A2_OFF, (unsigned)A2_BYTES);
  const unsigned so = ((mt * 2 + wm) * KS2 + 20 + (nb0 >> 1)) * 4096;
  const unsigned vo = lane * 16;
  float sc[4];
#pragma unroll
  for (int r = 0; r < 4; ++r) sc[r] = 1.f - *(const LAS float*)(uintptr_t)(K1_G + (wm * 64 + r * 16 + c) * 4);
#pragma unroll
  for (int j = 0; j < 3; ++j) {
    unsigned vj = vo + so + (wn * 4 + j) * 4096;
    if (j == 2 && wn == 1 && nb0 >= 84) vj = 0x40000000u;
    OPAQUE(vj);
#pragma unroll
    for (int r = 0; r < 4; ++r) {
      const f32x4 h0 = o.a.c[r][2 * j], h1 = o.a.c[r][2 * j + 1];
      bst16<ST_AUX>(ro, vj + r * 1024,
                    u32x4{mish2(h0[0], h0[1], sc[r]), mish2(h0[2], h0[3], sc[r]), mish2(h1[0], h1[1], sc[r]), mish2(h1[2], h1[3], sc[r])});
    }
    SCHED();
  }
  WAIT_V(4);
  if (lane == 0) sy_st(early, epoch);
  const unsigned v6 = so + 3 * 4096 + wn * 512 + (g & 1) * 1024 + (g >> 1) * 256 + c * 16;
#pragma unroll
  for (int r = 0; r < 4; r += 2) {
    const f32x4 h0 = o.a.c[r][6], h1 = o.a.c[r + 1][6];
    bst16<ST_AUX>(ro, v6 + r * 1024, widen_pair(u32x2{mish2(h0[0], h0[1], sc[r]), mish2(h0[2], h0[3], sc[r])},
                                                u32x2{mish2(h1[0], h1[1], sc[r + 1]), mish2(h1[2], h1[3], sc[r + 1])}));
  }
}

__device__ __forceinline__ int w2cat_row(int beta, int rho) {
  if (beta < 32) return 20 * beta + rho;
  if (beta < 40) return 20 * (4 * (beta - 32) + (rho >> 2)) + 16 + (rho & 3);
  return 16 * (beta - 40) + rho;
}

__device__ __forceinline__ int kb_of(int t) { return 20 + 7 * t - (t == 7); }
__device__ __forceinline__ int ks_front(int w, int n) {
  if (n < 3) { const int e = 20 + 3 * w + n; return kb_of(e >> 2) + (e & 1) + 2 * (e & 2); }
  if (n < 7) return kb_of(2 * w + ((n - 3) >> 1)) + 2 + ((n - 3) & 1);
  return 12 + 2 * w + n - 7;
}
__device__ __forceinline__ int ks_back(int lw, int n) {
  if (n < 5) return kb_of(n) + (lw & 1) + 2 * (lw & 2);
  const int q = 4 * (n - 5) + lw;
  return q < 6 ? kb_of(q) + 6 : (q < 18 ? q - 6 : 0x40000);
}
constexpr int P2_REG = 7 * 5120;
static_assert(4 * P2_REG <= EX_OFF, "phase 2 re-uses the phase 1 ring");

typedef u32x4 __attribute__((aligned(8))) u32x4_a8;
#define POLL_SLEEP 4
__device__ __forceinline__ void flags_wait(const gu64* f, const gu64* bs) {
  const u64 want = sy_ld(bs) + 1;
#pragma unroll 1
  for (int i = 0; i < SPIN_CAP; ++i) { if (__all(sy_ld(f) == want)) break; __builtin_amdgcn_s_sleep(POLL_SLEEP); }
}
__device__ __forceinline__ bool fb_up(const gu64* sy, int lane) {
  bool ok = true;
#pragma unroll
  for (int i = 0; i < 4; ++i) ok &= sy_ld(sy + SY_FB + 64 * i + lane) == sy_ld(sy + SY_BASE + 64 * i + lane) + 1;
  return __all(ok);
}
#define ACQUIRE() __builtin_amdgcn_fence(__ATOMIC_ACQUIRE, "wavefront")
__device__ __forceinline__ void ld16_dev(u32x4& r, unsigned voff, u32x4 rsrc, unsigned soff) {
  asm volatile("buffer_load_dwordx4 %0, %1, %2, %3 offen sc1" : "=v"(r) : "v"(voff), "s"(rsrc), "s"(soff) : "memory");
}
__device__ __forceinline__ void ld8_dev(u64& r, const gu64* p) { asm volatile("global_load_dwordx2 %0, %1, off sc1" : "=v"(r) : "v"(p) : "memory"); }
__device__ __forceinline__ void ld8_buf(u64& r, unsigned voff, u32x4 rsrc, unsigned soff) {
  asm volatile("buffer_load_dwordx2 %0, %1, %2, %3 offen sc1" : "=v"(r) : "v"(voff), "s"(rsrc), "s"(soff) : "memory");
}
#define PIN(x) asm volatile("" : "+v"(x))
template <int N> __device__ __forceinline__ void wait_v() { asm volatile("s_waitcnt vmcnt(%0)" :: "i"(N) : "memory"); }

__device__ __forceinline__ void mma20(f32x4 (&acc)[4][5], const u32x4 (&a)[4], const u32x4 (&b)[5]) {
#pragma unroll
  for (int i = 0; i < 4; ++i)
#pragma unroll
    for (int j = 0; j < 5; ++j) acc[i][j] = mfma16(b[j], a[i], acc[i][j]);
}
template <int N> __device__ __forceinline__ void wait_pin(u32x4 (&a)[4]) {
  asm volatile("s_waitcnt vmcnt(%4)" : "+v"(a[0]), "+v"(a[1]), "+v"(a[2]), "+v"(a[3]) : "i"(N) : "memory");
}

#define P2_NB 5
#define P2_NA 3
struct Stream2 {
  static constexpr int NB = P2_NB, NA = P2_NA, NS = 10;
  u32x4 rs;
  unsigned va[4], vo, sa, sb;
  int lw;
  __device__ __forceinline__ unsigned kstep(int n) const { return ks_back(lw, n); }
  u32x4 fb[NB][5], fa[NA][4];
  f32x4 acc[4][5];
  template <int N> __device__ __forceinline__ void load_b() {
    const unsigned so = sb + kstep(N) * 5120;
#pragma unroll
    for (int j = 0; j < 5; ++j) ld16_dev(fb[N % NB][j], vo, rs, so + j * 1024);
  }
  template <int N> __device__ __forceinline__ void load_a() {
    const unsigned so = sa + kstep(N) * 4096;
#pragma unroll
    for (int i = 0; i < 4; ++i) ld16_dev(fa[N % NA][i], va[i], rs, so);
  }
  static constexpr int behind(int m) { return m < 0 ? 0 : (m + NA < NS ? 4 : 0) + (m + NB < NS ? 5 : 0); }
  static constexpr int younger(int n) {
    int c = 0;
    if (n < NA) c += 4 * (NA - 1 - n);
    for (int m = (n < NA ? 0 : n - NA + 1); m < n; ++m) c += behind(m);
    return c;
  }
  template <int N> __device__ __forceinline__ void run() {
    if constexpr (N < NS) {
      wait_pin<younger(N)>(fa[N % NA]);
#pragma unroll
      for (int j = 0; j < 5; ++j) PIN(fb[N % NB][j]);
      mma20(acc, fa[N % NA], fb[N % NB]);
      SCHED();
      if constexpr (N + NB < NS) load_b<N + NB>();
      if constexpr (N + NA == 5) { lds_wait(0, 2u); ACQUIRE(); }
      if constexpr (N + NA == 6) { lds_wait(0, 3u); ACQUIRE(); }
      if constexpr (N + NA < NS) load_a<N + NA>();
      run<N + 1>();
    }
  }
};

__global__ __launch_bounds__(512, 2) void fused_kernel(const unsigned short* __restrict__ x, const unsigned short* __restrict__ wqkv,
                                                       const unsigned short* __restrict__ wo, const unsigned short* __restrict__ w1,
                                                       const unsigned short* __restrict__ b1, const unsigned short* __restrict__ w2,
                                                       const unsigned short* __restrict__ gate_w, unsigned char* ws,
                                                       unsigned short* __restrict__ out) {
  extern __shared__ __attribute__((aligned(16))) unsigned char smem[];
  const int lane = threadIdx.x & 63;
  const int wave = __builtin_amdgcn_readfirstlane(threadIdx.x >> 6);
  const int b = blockIdx.x, xcd = b & 7, idx = b >> 3;
  const int mt = (xcd & 1) * 8 + (idx & 7);
  const int nsel = idx >> 3;
  const int nt = (xcd >> 1) * 2 + (nsel & 1);
  const bool attn = nsel < 2;
  const int m0 = mt * 128;
  const int nb0 = nt < 7 ? nt * 14 : 96;
  const int L = mt * 16 + (attn ? 0 : 8) + nt;
  const int mt2 = (xcd & 3) * 8 + (idx & 7);
  const int nt2 = (xcd >> 2) * 4 + (idx >> 3);
  gu64* sy = (gu64*)(ws + SY_OFF);
  STAMP(ws, 0, 0);
  if (idx == 16 && wave < 4) {
    uint64_t pc;
    asm volatile("s_getpc_b64 %0" : "=s"(pc));
    const u32x4 rc = make_rsrc((const void*)(pc & ~(uint64_t)255), CODE_BYTES);
    dma_touch(K1_PF, (unsigned)(wave * 64 + lane) * 128u, rc, 0);
    dma_touch(K1_PF, (unsigned)(wave * 64 + lane) * 128u, rc, 32768);
  }

  if (__builtin_expect(wave >= 4, 1)) {
    const int lw = wave - 4;
    u64 pf[4], pb[4];
    {
      Stager st;
      st.rx = make_rsrc(x, S_TOK * D * 2);
      st.vx = (unsigned)((8 * lw + (lane >> 3)) * (D * 2) + (((lane & 7) ^ ((4 * lw + (lane >> 4)) & 7)) * 16));
      st.sx = (unsigned)(m0 * D * 2);
      st.lx = lw * 1024;
      stage_x(st, 0);
      st.rw = make_rsrc(attn ? wqkv : w1, attn ? D * 3 * D * 2 : D * F * 2);
      const unsigned rowbytes = attn ? 3 * D * 2 : F * 2, secstride = attn ? D * 2 - 160 : 0;
      const int chmax = attn ? 29 : (nt < 6 ? 27 : 23);
      st.wstep = 64u * rowbytes;
      st.sw = attn ? nt * 160 : nb0 * 32;
      st.lw = X_BYTES + lw * 1024;
#pragma unroll
      for (int j = 0; j < 7; ++j) st.vw[j] = w_src(lw + 4 * j, lane, rowbytes, secstride, chmax);
      {
        unsigned v4w = w_src(26 + lw, lane, rowbytes, secstride, chmax);
        const bool hx = lw < 2;
        st.v4 = hx ? st.vx : v4w;
        st.r4 = hx ? st.rx : st.rw;
        st.s4 = hx ? (attn ? (m0 ? st.sx - 16 * D * 2 : 0u) : 0x40000000u) : st.sw;
        st.step4 = hx ? 128u : st.wstep;
        st.l4 = hx ? (16 + lw) * 1024 : X_BYTES + (26 + lw) * 1024;
      }
      stage_w(st, 0); stage_next(st);
      stage_x(st, STAGE); stage_w(st, STAGE); stage_next(st);
      const u64 epl = *(const __attribute__((address_space(4))) u64*)(uintptr_t)(ws + SY_OFF + (SY_BASE + L) * 8) + 1;
      WAIT_V(12);
      STAMP(ws, 0, 1);
      BAR();
      const int unit = lw < 2 ? lw * 256 + b : 384 + b;
      const bool tile = lw < 2 || (lw == 2 && b >= 128 && b < 208);
      const int uks = unit >> 3;
      const unsigned lt = EX_OFF + lw * 5120;
      if (tile) {
        const u32x4 r2 = make_rsrc(uks < 20 ? wo : w2, uks < 20 ? D * D * 2 : F * D * 2);
#pragma unroll
        for (int i = 0; i < 5; ++i) {
          unsigned ci = i * 64 + lane, row = (ci * 205u) >> 11, ch = ci - row * 10u;
          dma16(lt + i * 1024, (unsigned)w2cat_row(2 * uks + (row >> 4), row & 15) * (D * 2) + ch * 16, r2, (unit & 7) * 160);
        }
      }
      stage_x(st, 2 * STAGE); stage_w(st, 2 * STAGE); stage_next(st);
      unsigned sb = 0;
#pragma unroll 1
      for (int t = 0; t < NT1 - 3; ++t) {
        if (tile && (t == 0 || t == 2)) WAIT_V(17); else WAIT_V(12);
        BAR();
        if (t == 3 && lw == 0 && lane == 0) sy_st(sy + SY_FB + L, epl);
        if (t == 6 && lw == 0) {
#pragma unroll
          for (int i = 0; i < 4; ++i) { ld8_dev(pf[i], sy + SY_FB + 64 * i + lane); ld8_dev(pb[i], sy + SY_BASE + 64 * i + lane); }
        }
        stage_x(st, sb); stage_w(st, sb); stage_next(st);
        if (t == 1 && tile) {
          const bool half = uks < 20 || uks == 71 || (uks < 66 && (uks - 23) % 7 == 0);
          const unsigned ra = lt + ((half ? 8 : 4) * (lane >> 4) + ((lane >> 2) & 3)) * 160 + (lane & 3) * 40;
          const unsigned rb = ra + (half ? 4 : 16) * 160;
          unsigned char* bp = ws + B2_OFF + ((size_t)(unit & 7) * KS2 + uks) * 5120 + lane * 16;
#pragma unroll
          for (int i = 0; i < 5; ++i) st16(bp + i * 1024, cat(lds_tr(ra + 8 * i), lds_tr(rb + 8 * i)));
        }
        sb += STAGE; if (sb == NSTAGE * STAGE) sb = 0;
      }
    }
    Stream2 ld;
    ld.rs = make_rsrc(ws, (unsigned)(A2_BYTES + B2_BYTES + 8192));
    ld.vo = lane * 16;
#pragma unroll
    for (int i = 0; i < 4; ++i) ld.va[i] = lane * 16 + ((i + lw) & 3) * 1024;
    const unsigned sb0 = (unsigned)B2_OFF + nt2 * (KS2 * 5120);
    ld.sa = (unsigned)A2_OFF + mt2 * (KS2 * 4096); ld.sb = sb0; ld.lw = lw;
    WAIT_V(12);
    if (lw == 0) {
      bool ok = true;
#pragma unroll
      for (int i = 0; i < 4; ++i) {
        PIN(pf[i]); PIN(pb[i]); ok &= pf[i] == pb[i] + 1;
        *(LAS u64*)(uintptr_t)(K1_BASES + i * 512 + lane * 8) = pb[i];
      }
      if (!__all(ok)) {
#pragma unroll 1
        for (int i = 0; i < SPIN_CAP; ++i) { if (fb_up(sy, lane)) break; __builtin_amdgcn_s_sleep(POLL_SLEEP); }
      }
    }
    BAR();
    ACQUIRE();
    if (lw == 1 || lw == 2)
      asm volatile("s_mov_b32 m0, %0\n\ts_nop 0\n\tbuffer_load_dword %1, %2, %3 offen sc1 lds"
                   :: "s"(K1_G + (lw - 1) * 256), "v"(lane * 4), "s"(ld.rs), "s"((unsigned)G_OFF + (m0 + (lw - 1) * 64) * 4) : "memory");
    ld.load_b<0>(); ld.load_b<1>(); ld.load_b<2>();
    WAIT_V(15); BAR();
    ld.load_b<3>(); ld.load_b<4>();
    if constexpr (Stream2::NB > 5) ld.load_b<5>();
    BAR();
    STAMP(ws, 0, 2);
    auto stage_b = [&](int n) {
      const int ks = ks_front(lw, n);
#pragma unroll
      for (int j = 0; j < 5; ++j) dma16_dev(lw * P2_REG + n * 5120 + j * 1024, ld.vo, ld.rs, sb0 + ks * 5120 + j * 1024);
    };
#define THIN 5
    stage_b(0); stage_b(1);
    wait_v<THIN>(); stage_b(2); wait_v<THIN>(); stage_b(3); wait_v<THIN>(); stage_b(4); wait_v<THIN>();
    const int Lp = (mt2 >> 1) * 16 + 8 + ((lane >> 1) & 7);
    const gu64* fm = sy + SY_FE + Lp * 4 + (mt2 & 1) * 2 + (lane & 1);
    stage_b(5); wait_v<THIN>(); stage_b(6);
    if (lw == 0) { ld8_dev(pf[0], fm); ld8_dev(pb[0], sy + SY_BASE + Lp); }
#pragma unroll
    for (int i = 0; i < 4; ++i)
#pragma unroll
      for (int j = 0; j < 5; ++j) ld.acc[i][j] = f32x4{0.f, 0.f, 0.f, 0.f};
    WAIT_V(0);
    lds_post(4 + lw, 1u, lane);
    STAMP(ws, 0, 3);
    if (lw == 0) {
      PIN(pf[0]); PIN(pb[0]);
#pragma unroll 1
      for (int k = 0; k < SPIN_CAP && !__all(pf[0] == pb[0] + 1); ++k) { __builtin_amdgcn_s_sleep(POLL_SLEEP); pf[0] = sy_ld(fm); }
      lds_post(0, 1u, lane);
    } else lds_wait(0, 1u);
    ACQUIRE();
    STAMP(ws, 0, 4);
    ld.load_a<0>(); ld.load_a<1>();
    if constexpr (Stream2::NA > 2) ld.load_a<2>();
    ld.run<0>();
    STAMP(ws, 0, 5);
    lds_wait(8 + lw, 1u);
    unsigned rr = lw * P2_REG + lane * 16;
    OPAQUE(rr);
#pragma unroll
    for (int i = 0; i < 4; ++i)
#pragma unroll
      for (int j = 0; j < 5; ++j)
        if (i > 0 || j < 2) *(LAS f32x4*)(uintptr_t)(rr + (i == 0 ? 18 + j : 15 + i * 5 + j) * 1024) = ld.acc[i][j];
    WAIT_L0();
    BAR();
    f32x4 r[3];
#pragma unroll
    for (int j = 2; j < 5; ++j) r[j - 2] = ld.acc[0][j] + *(const LAS f32x4*)(uintptr_t)(rr + (j - 2) * 1024);
#pragma unroll
    for (int d = 1; d < 4; ++d) {
      unsigned rq = ((lw - d) & 3) * P2_REG + lane * 16;
      OPAQUE(rq);
#pragma unroll
      for (int j = 2; j < 5; ++j)
        r[j - 2] += *(const LAS f32x4*)(uintptr_t)(rq + (d * 5 + j - 2) * 1024) + *(const LAS f32x4*)(uintptr_t)(rq + (15 + d * 5 + j) * 1024);
    }
    unsigned* op = (unsigned*)(out + (size_t)(mt2 * 64 + lw * 16 + (lane & 15)) * D + nt2 * 80 + (lane >> 4) * 20);
    *(u32x4_a8*)(op + 4) = u32x4{pack2(r[0][0], r[0][1]), pack2(r[0][2], r[0][3]), pack2(r[1][0], r[1][1]), pack2(r[1][2], r[1][3])};
    *(u32x2*)(op + 8) = u32x2{pack2(r[2][0], r[2][1]), pack2(r[2][2], r[2][3])};
    STAMP(ws, 0, 6);
    return;
  }

  const u64 epoch = *(const __attribute__((address_space(4))) u64*)(uintptr_t)(ws + SY_OFF + (SY_BASE + L) * 8) + 1;
  if (threadIdx.x < 16) *(LAS unsigned*)(uintptr_t)(K1_NEWS + threadIdx.x * 4) = 0u;
  u32x2 bias[7];
  if (!attn) {
    const u32x2* bp = (const u32x2*)(b1 + (lane >> 4) * 4);
#pragma unroll
    for (int n = 0; n < 7; ++n) bias[n] = bp[min(nb0 + (n < 6 ? (wave & 1) * 8 + n : 6 + (wave & 1)), 107) * 4];
  }
  {
    const int tok = L * 8 + wave * 2 + (lane >> 5);
    const u32x2* gp = (const u32x2*)gate_w + (lane & 31);
    const u32x2* xr = (const u32x2*)(x + (size_t)tok * D) + (lane & 31);
    float sg = 0.f;
#pragma unroll
    for (int j = 0; j < 5; ++j) {
      const u32x2 a = xr[32 * j], w = gp[32 * j];
      sg = fmaf(bf_lo(a.x), bf_lo(w.x), sg); sg = fmaf(bf_hi(a.x), bf_hi(w.x), sg);
      sg = fmaf(bf_lo(a.y), bf_lo(w.y), sg); sg = fmaf(bf_hi(a.y), bf_hi(w.y), sg);
    }
#pragma unroll
    for (int o = 16; o >= 1; o >>= 1) sg += __shfl_xor(sg, o, 64);
    if ((lane & 31) == 0)
      __hip_atomic_store((__attribute__((address_space(1))) float*)(ws + G_OFF) + tok, 1.f / (1.f + __expf(-sg)),
                         __ATOMIC_RELAXED, __HIP_MEMORY_SCOPE_AGENT);
  }
  if (!attn) {
#pragma unroll
    for (int n = 0; n < 7; ++n) PIN(bias[n]);
  }
#define PF_ATTN 0
  if (attn == (PF_ATTN != 0)) {
    const int wg = b & 127;
    const int pw = wg * 4 + wave;
    const int li = pw * 28 + (lane < 28 ? lane : 0);
    dma_touch(K1_PF, (unsigned)((li / 7) * (D * 2) + (3 + li % 7) * 128), make_rsrc(x, S_TOK * D * 2), 0);
    {
      const int q = ((idx & 15) * 4 + wave) * 63 + (lane < 63 ? lane : 0), row = q / 9, sl = q - row * 9;
      dma_touch(K1_PF, (unsigned)(row * (3 * D * 2) + (sl / 3) * (D * 2) + (sl % 3) * 128 + (xcd >> 1) * 320), make_rsrc(wqkv, D * 3 * D * 2), 192 * 3 * D * 2);
    }
    dma_touch(K1_PF, (unsigned)((pw * 24 + (lane < 24 ? lane : 0)) * 128), make_rsrc(w1, D * F * 2), 192 * F * 2);
  }
  WAIT_L0();
  if (attn) {
    AttnPacked pk;
    if (wave == 0) attn_main<0>(lane, pk, ws);
    else if (wave == 1) attn_main<1>(lane, pk, ws);
    else if (wave == 2) attn_main<2>(lane, pk, ws);
    else attn_main<3>(lane, pk, ws);
    attn_epilogue(lane, wave, mt, nt, pk, ws);
  } else {
    mlp_role(lane, wave, mt, nb0, bias, ws, sy + SY_FE + L * 4 + wave, epoch);
  }
  WAIT_V(0);
  if (lane == 0) sy_st(sy + SY_FA + L * 4 + wave, epoch);
  STAMP(ws, 0, 3);

  int ln = lane;
  OPAQUE(ln);
  const u32x4 rs = make_rsrc(ws, (unsigned)SY_OFF + 32768);
  const unsigned sa = (unsigned)A2_OFF + mt2 * (KS2 * 4096);
  unsigned va[4];
#pragma unroll
  for (int i = 0; i < 4; ++i) va[i] = ln * 16 + ((i + wave) & 3) * 1024;
  u32x4 fa[7][4], ea[2][4], eb[2][5];
  const int La = (mt2 >> 1) * 16 + (ln < 32 ? (ln >> 2) : 8 + ((ln >> 1) & 7));
  const gu64* fat = sy + SY_FA + La * 4 + (ln < 32 ? (ln & 3) : (mt2 & 1) * 2 + (ln & 1));
  u64 pp[4];
  u64 up = 0;
  lds_wait(4 + wave, 1u);
  lds_wait(0, 1u);
  ACQUIRE();
  if (wave == 0) {
    ld8_dev(pp[0], fat); ld8_dev(pp[1], sy + SY_BASE + La);
  }
#pragma unroll
  for (int n = 0; n < 3; ++n)
#pragma unroll
    for (int i = 0; i < 4; ++i) ld16_dev(fa[n][i], va[i], rs, sa + ks_front(wave, n) * 4096);
  if (wave == 0) {
    WAIT_V(12);
    PIN(pp[0]); PIN(pp[1]);
    up = __ballot(pp[0] == pp[1] + 1);
#pragma unroll 1
    for (int k = 0; k < SPIN_CAP && (up >> 32) != 0xffffffffull; ++k) { __builtin_amdgcn_s_sleep(POLL_SLEEP); up = __ballot(sy_ld(fat) == pp[1] + 1); }
    lds_post(0, up == ~0ull ? 3u : 2u, ln);
  }
  f32x4 acc[4][5];
#pragma unroll
  for (int i = 0; i < 4; ++i)
#pragma unroll
    for (int j = 0; j < 5; ++j) acc[i][j] = f32x4{0.f, 0.f, 0.f, 0.f};
  STAMP(ws, 0, 4);
  unsigned rb = wave * P2_REG + ln * 16;
  OPAQUE(rb);
  const unsigned sbb = (unsigned)B2_OFF + nt2 * (KS2 * 5120), vo = ln * 16;
#define P2_STEP(n, CNT) { u32x4 fb[5]; _Pragma("unroll") for (int j = 0; j < 5; ++j) fb[j] = lds_read128(rb + n * 5120 + j * 1024); \
    wait_pin<CNT>(fa[n]); mma20(acc, fa[n], fb); SCHED(); }
#define P2_EXTRA_B(e) { _Pragma("unroll") for (int j = 0; j < 5; ++j) ld16_dev(eb[e][j], vo, rs, sbb + ks_front(wave, 7 + e) * 5120 + j * 1024); }
#define P2_EXTRA_A(e) { _Pragma("unroll") for (int i = 0; i < 4; ++i) ld16_dev(ea[e][i], va[i], rs, sa + ks_front(wave, 7 + e) * 4096); }
  P2_STEP(0, 8)
  lds_wait(0, 2u);
  ACQUIRE();
#pragma unroll
  for (int n = 3; n < 7; ++n)
#pragma unroll
    for (int i = 0; i < 4; ++i) ld16_dev(fa[n][i], va[i], rs, sa + ks_front(wave, n) * 4096);
  P2_STEP(1, 20)
  if (wave == 0) {
#pragma unroll 1
    for (int k = 0; k < SPIN_CAP && up != ~0ull; ++k) { up = __ballot(sy_ld(fat) == pp[1] + 1); if (up != ~0ull) __builtin_amdgcn_s_sleep(POLL_SLEEP); }
    lds_post(0, 3u, ln);
    if (ln == 0) sy_st(sy + SY_PASS + L, epoch);
  }
  P2_EXTRA_B(0)
  P2_STEP(2, 21)
  P2_EXTRA_B(1)
  P2_STEP(3, 22) P2_STEP(4, 18)
  lds_wait(0, 3u);
  ACQUIRE();
  P2_EXTRA_A(0) P2_EXTRA_A(1)
  P2_STEP(5, 22) P2_STEP(6, 18)
#pragma unroll
  for (int e = 0; e < 2; ++e) {
    if (e == 0) wait_pin<4>(ea[0]); else wait_pin<0>(ea[1]);
#pragma unroll
    for (int j = 0; j < 5; ++j) PIN(eb[e][j]);
    mma20(acc, ea[e], eb[e]);
    SCHED();
  }
#undef P2_STEP
#undef P2_EXTRA_A
#undef P2_EXTRA_B
  if (wave == 0) {
#pragma unroll
    for (int i = 0; i < 4; ++i) ld8_buf(pp[i], ln * 8, rs, (unsigned)SY_OFF + (SY_PASS + 64 * i) * 8);
  }
#pragma unroll
  for (int i = 0; i < 4; ++i)
#pragma unroll
    for (int j = 0; j < 5; ++j)
      if (i > 0 || j >= 2) *(LAS f32x4*)(uintptr_t)(rb + (i * 5 + j - 2) * 1024) = acc[i][j];
  WAIT_L0();
  lds_post(8 + wave, 1u, ln);
  STAMP(ws, 0, 5);
  BAR();
  {
    f32x4 r[2];
#pragma unroll
    for (int j = 0; j < 2; ++j) r[j] = acc[0][j] + *(const LAS f32x4*)(uintptr_t)(rb + (18 + j) * 1024);
#pragma unroll
    for (int d = 1; d < 4; ++d) {
      unsigned rq = ((wave - d) & 3) * P2_REG + ln * 16;
      OPAQUE(rq);
#pragma unroll
      for (int j = 0; j < 2; ++j)
        r[j] += *(const LAS f32x4*)(uintptr_t)(rq + (d * 5 + j - 2) * 1024) + *(const LAS f32x4*)(uintptr_t)(rq + (15 + d * 5 + j) * 1024);
    }
    if (wave == 0) WAIT_V(0);
    unsigned* op = (unsigned*)(out + (size_t)(mt2 * 64 + wave * 16 + (ln & 15)) * D + nt2 * 80 + (ln >> 4) * 20);
    *(u32x4_a8*)op = u32x4{pack2(r[0][0], r[0][1]), pack2(r[0][2], r[0][3]), pack2(r[1][0], r[1][1]), pack2(r[1][2], r[1][3])};
  }
  if (wave == 0) {
    u64 wb[4];
#pragma unroll
    for (int i = 0; i < 4; ++i) { PIN(pp[i]); wb[i] = *(const LAS u64*)(uintptr_t)(K1_BASES + i * 512 + ln * 8); }
#pragma unroll 1
    for (int k = 0; k < SPIN_CAP; ++k) {
      if (__all(pp[0] == wb[0] + 1 && pp[1] == wb[1] + 1 && pp[2] == wb[2] + 1 && pp[3] == wb[3] + 1)) break;
      __builtin_amdgcn_s_sleep(POLL_SLEEP);
#pragma unroll
      for (int i = 0; i < 4; ++i) pp[i] = sy_ld(sy + SY_PASS + 64 * i + ln);
    }
    if (ln == 0) sy_st(sy + SY_BASE + L, epoch);
  }
  STAMP(ws, 0, 6);
}

}

extern "C" void kernel_launch(void* const* d_in, const int* in_sizes, int n_in, void* d_out, int out_size, void* d_ws,
                              size_t ws_size, hipStream_t stream) {
  const unsigned short* x = (const unsigned short*)d_in[0];
  const unsigned short* wqkv = (const unsigned short*)d_in[1];
  const unsigned short* wo = (const unsigned short*)d_in[2];
  const unsigned short* w1 = (const unsigned short*)d_in[4];
  const unsigned short* b1 = (const unsigned short*)d_in[5];
  const unsigned short* w2 = (const unsigned short*)d_in[6];
  const unsigned short* gate_w = (const unsigned short*)d_in[7];
  (void)hipFuncSetAttribute((const void*)fused_kernel, hipFuncAttributeMaxDynamicSharedMemorySize, K1_LDS);
  fused_kernel<<<256, 512, K1_LDS, stream>>>(x, wqkv, wo, w1, b1, w2, gate_w, (unsigned char*)d_ws, (unsigned short*)d_out);
}
```

```cpp
#include <hip/hip_runtime.h>
#include <hip/hip_bf16.h>
#include <stdint.h>

#define STAMP(ws, kern, i)
#define CODE_BYTES 34816

namespace {

typedef __attribute__((ext_vector_type(4))) short s16x4;
typedef __attribute__((ext_vector_type(8))) __bf16 bf16x8;
typedef __attribute__((ext_vector_type(2))) __bf16 bf16x2;
typedef __attribute__((ext_vector_type(4))) float f32x4;
typedef __attribute__((ext_vector_type(2))) float f32x2;
typedef __attribute__((ext_vector_type(4))) unsigned u32x4;
typedef __attribute__((ext_vector_type(2))) unsigned u32x2;
#define LAS __attribute__((address_space(3)))

constexpr int S_TOK = 2048, D = 640, F = 1728;
constexpr int KS2 = 74;
constexpr size_t A2_BYTES = (size_t)128 * KS2 * 1024;
constexpr size_t B2_BYTES = (size_t)40 * KS2 * 1024;
constexpr size_t A2_OFF = 0, B2_OFF = A2_OFF + A2_BYTES, G_OFF = B2_OFF + B2_BYTES;
constexpr size_t SY_OFF = G_OFF + 8192;
constexpr int SY_BASE = 0, SY_PASS = 256, SY_FB = 512, SY_FA = 1024, SY_FE = 2048;

constexpr int X_BYTES = 144 * 128;
constexpr int W_ROW = 480;
constexpr int W_BYTES = 64 * W_ROW;
constexpr int STAGE = X_BYTES + W_BYTES;
constexpr int NSTAGE = 3;
constexpr int EX_OFF = NSTAGE * STAGE;
constexpr int EX_Q = EX_OFF, EX_K = EX_Q + 8 * 512, EX_V = EX_K + 9 * 512;
constexpr int K1_PF = EX_OFF + 3 * 5120;
constexpr int K1_BASES = EX_V + 9 * 512;
static_assert(K1_BASES + 2048 <= K1_PF, "LDS map");
constexpr int K1_NEWS = K1_PF + 256;
constexpr int K1_G = K1_NEWS + 64;
constexpr int K1_LDS = K1_G + 512;
constexpr int NT1 = 10;

__device__ __forceinline__ unsigned pack2(float a, float b) {
  f32x2 v = {a, b};
  bf16x2 r = __builtin_convertvector(v, bf16x2);
  return __builtin_bit_cast(unsigned, r);
}
__device__ __forceinline__ float bf_lo(unsigned u) { return __builtin_bit_cast(float, u << 16); }
__device__ __forceinline__ float bf_hi(unsigned u) { return __builtin_bit_cast(float, u & 0xffff0000u); }
__device__ __forceinline__ f32x4 mfma16(u32x4 a, u32x4 b, f32x4 c) {
  return __builtin_amdgcn_mfma_f32_16x16x32_bf16(__builtin_bit_cast(bf16x8, a), __builtin_bit_cast(bf16x8, b), c, 0, 0, 0);
}
__device__ __forceinline__ u32x4 lds_read128(unsigned addr) { return *(const LAS u32x4*)(uintptr_t)addr; }
__device__ __forceinline__ u32x2 lds_read64(unsigned addr) { return *(const LAS u32x2*)(uintptr_t)addr; }
__device__ __forceinline__ u32x2 lds_tr(unsigned addr) {
  s16x4 v = __builtin_amdgcn_ds_read_tr16_b64_v4i16((LAS s16x4*)(uintptr_t)addr);
  return __builtin_bit_cast(u32x2, v);
}
__device__ __forceinline__ u32x4 lds_wfrag(unsigned addr) {
  u32x2 a = lds_tr(addr), b = lds_tr(addr + 8 * W_ROW);
  u32x4 r = {a.x, a.y, b.x, b.y};
  return r;
}
__device__ __forceinline__ void dma16(unsigned ldsaddr, unsigned voff, u32x4 rsrc, unsigned soff) {
  asm volatile("s_mov_b32 m0, %0\n\ts_nop 0\n\tbuffer_load_dwordx4 %1, %2, %3 offen lds"
               :: "s"(ldsaddr), "v"(voff), "s"(rsrc), "s"(soff) : "memory");
}
__device__ __forceinline__ void dma16_dev(unsigned ldsaddr, unsigned voff, u32x4 rsrc, unsigned soff) {
  asm volatile("s_mov_b32 m0, %0\n\ts_nop 0\n\tbuffer_load_dwordx4 %1, %2, %3 offen sc1 lds"
               :: "s"(ldsaddr), "v"(voff), "s"(rsrc), "s"(soff) : "memory");
}
#define PF_FLAGS ""
__device__ __forceinline__ void dma_touch(unsigned ldsaddr, unsigned voff, u32x4 rsrc, unsigned soff) {
  asm volatile("s_mov_b32 m0, %0\n\ts_nop 0\n\tbuffer_load_dword %1, %2, %3 offen " PF_FLAGS " lds"
               :: "s"(ldsaddr), "v"(voff), "s"(rsrc), "s"(soff) : "memory");
}
#define ST_FLAGS "sc1"
__device__ __forceinline__ void st16(void* p, u32x4 v) {
  asm volatile("global_store_dwordx4 %0, %1, off " ST_FLAGS "\n\ts_nop 1" :: "v"(p), "v"(v) : "memory");
}
__device__ __forceinline__ void st8(void* p, u32x2 v) {
  asm volatile("global_store_dwordx2 %0, %1, off " ST_FLAGS :: "v"(p), "v"(v) : "memory");
}
#define ST_AUX 16
typedef __amdgpu_buffer_rsrc_t rsrc_t;
__device__ __forceinline__ rsrc_t out_rsrc(void* p, unsigned bytes) { return __builtin_amdgcn_make_buffer_rsrc(p, 0, bytes, 0x00020000); }
template <int AUX> __device__ __forceinline__ void bst16(rsrc_t r, unsigned voff, u32x4 v) { __builtin_amdgcn_raw_buffer_store_b128(v, r, voff, 0, AUX); }
template <int AUX> __device__ __forceinline__ void bst8(rsrc_t r, unsigned voff, unsigned soff, u32x2 v) { __builtin_amdgcn_raw_buffer_store_b64(v, r, voff, soff, AUX); }
#define WAIT_V(n) asm volatile("s_waitcnt vmcnt(" #n ")" ::: "memory")
#define WAIT_L0() __builtin_amdgcn_s_waitcnt(0xC07F)
#define OPAQUE(x) asm volatile("" : "+v"(x))
#define BAR() __builtin_amdgcn_s_barrier()
#define SCHED() __builtin_amdgcn_sched_barrier(0)

__device__ __forceinline__ u32x4 make_rsrc(const void* p, unsigned bytes) {
  uint64_t a = (uint64_t)p;
  u32x4 r;
  r.x = __builtin_amdgcn_readfirstlane((unsigned)a);
  r.y = __builtin_amdgcn_readfirstlane((unsigned)(a >> 32) & 0xffffu);
  r.z = bytes;
  r.w = 0x00020000u;
  return r;
}

__device__ __forceinline__ int swap23(int k) { return (k & ~12) | ((k & 4) << 1) | ((k & 8) >> 1); }

struct Stager {
  unsigned vx, vw[7], v4;
  u32x4 rx, rw, r4;
  unsigned sx, sw, s4;
  unsigned wstep, step4;
  unsigned lx, lw, l4;
};
__device__ __forceinline__ void stage_x(const Stager& st, unsigned sb) {
#pragma unroll
  for (int p = 0; p < 4; ++p) dma16(sb + st.lx + p * 4096, st.vx, st.rx, st.sx + p * (32 * D * 2));
}
__device__ __forceinline__ void stage_w(const Stager& st, unsigned sb) {
#pragma unroll
  for (int j = 0; j < 7; ++j) dma16(sb + st.lw + j * 4096, st.vw[j], st.rw, st.sw);
  dma16(sb + st.l4, st.v4, st.r4, st.s4);
}
__device__ __forceinline__ void stage_next(Stager& st) { st.sx += 128u; st.sw += st.wstep; st.s4 += st.step4; }
__device__ __forceinline__ unsigned w_src(int wi, int lane, unsigned rowbytes, unsigned secstride, int chmax) {
  unsigned ci = wi * 64 + lane;
  unsigned pos = (ci * 2185u) >> 16, ch = ci - pos * 30u;
  unsigned sec = (ch * 205u) >> 11;
  unsigned k = swap23(pos);
  ch = ch < (unsigned)chmax ? ch : (unsigned)chmax;
  return k * rowbytes + ch * 16u + sec * secstride;
}

struct AttnAcc { f32x4 q[8], k[9], v[9], e[9]; };
__device__ __forceinline__ u32x4 cat(u32x2 a, u32x2 b) { return u32x4{a.x, a.y, b.x, b.y}; }

template <int W>
struct AttnOps {
  struct Frags { u32x4 x[9]; u32x2 wl[4], wh[4]; };
  AttnAcc a;
  unsigned xa, wa, we;
  static constexpr int per_rb(int rb) { return 2 + (rb < 8 ? 1 : 0) + ((W == 3 || (W == 0 && rb == 8)) ? 0 : 1); }
  static constexpr int NM = per_rb(8) + 8 * per_rb(0);
  static constexpr int NR = W < 3 ? 17 : 15;
  template <int I> __device__ __forceinline__ void mma(const Frags& f) {
    constexpr int rb = I < per_rb(8) ? 8 : (I - per_rb(8)) / per_rb(0);
    constexpr int j = I < per_rb(8) ? I : (I - per_rb(8)) % per_rb(0);
    constexpr bool has_e = !(W == 3 || (W == 0 && rb == 8));
    if constexpr (j == 0) a.k[rb] = mfma16(cat(f.wl[1], f.wh[1]), f.x[rb], a.k[rb]);
    else if constexpr (j == 1) a.v[rb] = mfma16(f.x[rb], cat(f.wl[2], f.wh[2]), a.v[rb]);
    else if constexpr (j == 2 && has_e) {
      if constexpr (W == 2) a.e[rb] = mfma16(f.x[rb], cat(f.wl[3], f.wh[3]), a.e[rb]);
      else a.e[rb] = mfma16(cat(f.wl[3], f.wh[3]), f.x[rb], a.e[rb]);
    } else a.q[rb] = mfma16(cat(f.wl[0], f.wh[0]), f.x[rb], a.q[rb]);
  }
  unsigned x, w, e;
  __device__ __forceinline__ void bases(unsigned sb, int kk) {
    x = (xa ^ (kk * 64)) + sb; w = wa + sb + kk * 32 * W_ROW; e = we + sb + kk * 32 * W_ROW;
    OPAQUE(x); OPAQUE(w); if constexpr (W < 3) OPAQUE(e);
  }
  template <int I0> __device__ __forceinline__ void read(Frags& f) {
    constexpr int I = (W == 3 && I0 >= 5) ? I0 + 2 : I0;
    if constexpr (I == 0) f.wl[1] = lds_tr(w + 160);
    if constexpr (I == 1) f.wh[1] = lds_tr(w + 160 + 8 * W_ROW);
    if constexpr (I == 2) f.x[8] = lds_read128(x + 8 * 2048);
    if constexpr (I == 3) f.wl[2] = lds_tr(w + 320);
    if constexpr (I == 4) f.wh[2] = lds_tr(w + 320 + 8 * W_ROW);
    if constexpr (I == 5) f.wl[3] = lds_tr(e);
    if constexpr (I == 6) f.wh[3] = lds_tr(e + 8 * W_ROW);
    if constexpr (I == 7) f.x[0] = lds_read128(x);
    if constexpr (I == 8) f.wl[0] = lds_tr(w);
    if constexpr (I == 9) f.wh[0] = lds_tr(w + 8 * W_ROW);
    if constexpr (I >= 10) f.x[I - 9] = lds_read128(x + (I - 9) * 2048);
  }
};

struct MlpAcc { f32x4 c[4][7]; };
struct MlpOps {
  struct Frags { u32x4 x[4]; u32x2 wl[7], wh[7]; };
  MlpAcc a;
  unsigned xa, wa;
  static constexpr int NM = 28, NR = 18;
  template <int I> __device__ __forceinline__ void mma(const Frags& f) {
    constexpr int n = I / 4, r = I % 4;
    a.c[r][n] = mfma16(cat(f.wl[n], f.wh[n]), f.x[r], a.c[r][n]);
  }
  unsigned x, w, w6, d6;
  __device__ __forceinline__ void bases(unsigned sb, int kk) {
    x = (xa ^ (kk * 64)) + sb; w = wa + sb + kk * 32 * W_ROW; w6 = w + d6;
    OPAQUE(x); OPAQUE(w); OPAQUE(w6);
  }
  template <int I> __device__ __forceinline__ void read(Frags& f) {
    if constexpr (I < 2) { if constexpr (I == 0) f.wl[0] = lds_tr(w); else f.wh[0] = lds_tr(w + 8 * W_ROW); }
    else if constexpr (I < 6) f.x[I - 2] = lds_read128(x + (I - 2) * 2048);
    else {
      constexpr int n = 1 + (I - 6) / 2;
      const unsigned a = n < 6 ? w + 32 * n : w6;
      if constexpr ((I & 1) == 0) f.wl[n] = lds_tr(a); else f.wh[n] = lds_tr(a + 8 * W_ROW);
    }
  }
};

template <class OPS, int I>
__device__ __forceinline__ void read_all(OPS& o, typename OPS::Frags& f) {
  if constexpr (I < OPS::NR) { o.template read<I>(f); read_all<OPS, I + 1>(o, f); }
}
template <class OPS, int I>
__device__ __forceinline__ void half_step(OPS& o, const typename OPS::Frags& fc, typename OPS::Frags& fn) {
  if constexpr (I < OPS::NM) {
    o.template mma<I>(fc);
#define RD_TAIL 6
    constexpr int span = OPS::NM - RD_TAIL;
    static_assert(span >= OPS::NR, "one read per gap");
    constexpr int j = (I * OPS::NR + span - 1) / span;
    if constexpr (j < OPS::NR && j * span / OPS::NR == I) { SCHED(); o.template read<j>(fn); }
    SCHED();
    half_step<OPS, I + 1>(o, fc, fn);
  }
}

template <class OPS>
__device__ __forceinline__ void k_loop(OPS& o) {
  typename OPS::Frags f0, f1;
  o.bases(0, 0);
  BAR();
  read_all<OPS, 0>(o, f0);
  unsigned sb = 0;
#pragma unroll 1
  for (int t = 0; t < NT1; ++t) {
    unsigned sn = sb + STAGE; if (sn == NSTAGE * STAGE) sn = 0;
    o.bases(sb, 1);
    WAIT_L0();
    SCHED();
    half_step<OPS, 0>(o, f0, f1);
    o.bases(sn, 0);
    WAIT_L0();
    if (t == 2) WAIT_V(0);
    BAR();
    SCHED();
    half_step<OPS, 0>(o, f1, f0);
    sb = sn;
  }
}

__device__ __forceinline__ float fmx(float a, float b) { return __builtin_elementwise_maximum(a, b); }
__device__ __forceinline__ void swap32(float& a, float& b) {
  auto r = __builtin_amdgcn_permlane32_swap(__builtin_bit_cast(unsigned, a), __builtin_bit_cast(unsigned, b), false, false);
  unsigned r0 = r[0], r1 = r[1];
  a = __builtin_bit_cast(float, r0); b = __builtin_bit_cast(float, r1);
}
__device__ __forceinline__ void swap16(float& a, float& b) {
  auto r = __builtin_amdgcn_permlane16_swap(__builtin_bit_cast(unsigned, a), __builtin_bit_cast(unsigned, b), false, false);
  unsigned r0 = r[0], r1 = r[1];
  a = __builtin_bit_cast(float, r0); b = __builtin_bit_cast(float, r1);
}
__device__ __forceinline__ u32x4 widen_pair(u32x2 a, u32x2 b) {
  auto x = __builtin_amdgcn_permlane16_swap(a.x, b.x, false, false);
  auto y = __builtin_amdgcn_permlane16_swap(a.y, b.y, false, false);
  unsigned x0 = x[0], x1 = x[1], y0 = y[0], y1 = y[1];
  return u32x4{x0, y0, x1, y1};
}
template <bool MAX>
__device__ __forceinline__ float reduce4(float x0, float x1, float x2, float x3) {
  swap32(x0, x1); swap32(x2, x3);
  float y01 = MAX ? fmx(x0, x1) : x0 + x1, y23 = MAX ? fmx(x2, x3) : x2 + x3;
  swap16(y01, y23);
  return MAX ? fmx(y01, y23) : y01 + y23;
}
__device__ __forceinline__ void bcast4(float z, float& x0, float& x1, float& x2, float& x3) {
  float u = z, w = z;
  swap16(u, w);
  x0 = u; x1 = u; swap32(x0, x1);
  x2 = w; x3 = w; swap32(x2, x3);
}

constexpr int SPIN_CAP = 1 << 16;
__device__ __forceinline__ void lds_wait(int word, unsigned atleast) {
  volatile LAS unsigned* p = (volatile LAS unsigned*)(uintptr_t)(K1_NEWS + word * 4);
#pragma unroll 1
  for (int i = 0; i < 16 * SPIN_CAP; ++i) { if (__builtin_amdgcn_readfirstlane(*p) >= atleast) break; __builtin_amdgcn_s_sleep(1); }
}
__device__ __forceinline__ void lds_post(int word, unsigned v, int lane) {
  if (lane == 0) *(volatile LAS unsigned*)(uintptr_t)(K1_NEWS + word * 4) = v;
}

struct AttnPacked { u32x2 q[8], k[9], v[9]; };

template <int W>
__device__ __forceinline__ void attn_main(int lane, AttnPacked& pk, unsigned char* __restrict__ ws) {
  OPAQUE(lane);
  const int c = lane & 15, g = lane >> 4, q4 = (lane >> 2) & 3, p4 = lane & 3;
  AttnOps<W> o;
#pragma unroll
  for (int i = 0; i < 9; ++i) {
    if (i < 8) o.a.q[i] = f32x4{0.f, 0.f, 0.f, 0.f};
    o.a.k[i] = f32x4{0.f, 0.f, 0.f, 0.f}; o.a.v[i] = f32x4{0.f, 0.f, 0.f, 0.f}; o.a.e[i] = f32x4{0.f, 0.f, 0.f, 0.f};
  }
  o.xa = c * 128 + ((g ^ ((c >> 1) & 7)) * 16);
  const unsigned wrow = X_BYTES + ((g >> 1) * 16 + (g & 1) * 4 + q4) * W_ROW;
  o.wa = wrow + 40 * W + 8 * p4;
  o.we = wrow + 160 * (W < 3 ? W : 0) + 40 * p4 + 32;
  STAMP(ws, 0, 1);
  k_loop(o);
  STAMP(ws, 0, 2);
  OPAQUE(lane);
  if constexpr (W < 3) {
    constexpr int base = W == 0 ? EX_Q : (W == 1 ? EX_K : EX_V);
#pragma unroll
    for (int i = 0; i < (W == 0 ? 8 : 9); ++i) {
      u32x2 v = {pack2(o.a.e[i][0], o.a.e[i][1]), pack2(o.a.e[i][2], o.a.e[i][3])};
      *(LAS u32x2*)(uintptr_t)(base + i * 512 + lane * 8) = v;
    }
  }
#pragma unroll
  for (int i = 0; i < 9; ++i) {
    if (i < 8) pk.q[i] = u32x2{pack2(o.a.q[i][0], o.a.q[i][1]), pack2(o.a.q[i][2], o.a.q[i][3])};
    pk.k[i] = u32x2{pack2(o.a.k[i][0], o.a.k[i][1]), pack2(o.a.k[i][2], o.a.k[i][3])};
    pk.v[i] = u32x2{pack2(o.a.v[i][0], o.a.v[i][1]), pack2(o.a.v[i][2], o.a.v[i][3])};
  }
}

__device__ __forceinline__ void attn_epilogue(int lane, int W, int mt, int hg, AttnPacked& pk, unsigned char* __restrict__ ws) {
  OPAQUE(lane);
  const int c = lane & 15, g = lane >> 4;
  f32x4 mprev, mcur, mfirst;
#pragma unroll
  for (int r = 0; r < 4; ++r) {
    int dcur = c - 4 * g - r, dprev = dcur + 16;
    mcur[r] = (dcur >= 0 && (dcur & 1) == 0) ? 0.f : -1e30f;
    mprev[r] = (dprev <= 14 && (dprev & 1) == 0) ? 0.f : -1e30f;
    mfirst[r] = mt == 0 ? -1e30f : mprev[r];
  }
  const float c2 = 0.22360679774997896f * 1.4426950408889634f;
  const unsigned sel_lo = (c & 1) ? 0x05040c0cu : 0x0c0c0504u, sel_hi = (c & 1) ? 0x07060c0cu : 0x0c0c0706u;
  unsigned exa = EX_OFF + W * 128 + c * 8 + g * 2, exv = EX_V + (g * 16 + W * 4 + (c & 3)) * 8;
  unsigned exg = K1_G + c * 4;
  OPAQUE(exa); OPAQUE(exv); OPAQUE(exg);
  WAIT_L0();
  if (W < 3 && lane == 0) __hip_atomic_fetch_add((LAS unsigned*)(uintptr_t)(K1_NEWS + 4), 1u, __ATOMIC_RELAXED, __HIP_MEMORY_SCOPE_WORKGROUP);
#pragma unroll
  for (int i = 0; i < 9; ++i) { if (i < 8) OPAQUE(pk.q[i]); OPAQUE(pk.k[i]); OPAQUE(pk.v[i]); }
  OPAQUE(mprev); OPAQUE(mcur); OPAQUE(mfirst);
  lds_wait(1, 3u);
  asm volatile("" ::: "memory");
  unsigned kx[9], qx[8]; u32x2 vx[9];
#pragma unroll
  for (int i = 0; i < 9; ++i) {
    kx[i] = *(const LAS unsigned short*)(uintptr_t)(exa + (EX_K - EX_OFF) + i * 512);
    if (i < 8) qx[i] = *(const LAS unsigned short*)(uintptr_t)(exa + (EX_Q - EX_OFF) + i * 512);
  }
#pragma unroll
  for (int i = 0; i < 9; ++i) vx[i] = lds_read64(exv + i * 512);
  float gt[8];
#pragma unroll
  for (int i = 0; i < 8; ++i) gt[i] = *(const LAS float*)(uintptr_t)(exg + i * 64);
  float t[8][4], m[8];
  f32x4 sp_[8], sc_[8];
#pragma unroll
  for (int i = 0; i < 8; ++i) {
    const int ip = i == 0 ? 8 : i - 1;
    const u32x4 qf = {pk.q[i].x, pk.q[i].y, qx[i], 0u};
    sp_[i] = mfma16(u32x4{pk.k[ip].x, pk.k[ip].y, kx[ip], 0u}, qf, i == 0 ? mfirst : mprev);
    sc_[i] = mfma16(u32x4{pk.k[i].x, pk.k[i].y, kx[i], 0u}, qf, mcur);
  }
  SCHED();
#pragma unroll
  for (int i = 0; i < 8; ++i) {
    const f32x4 sp = sp_[i], sc = sc_[i];
    t[i][0] = fmx(sp[0], sp[1]); t[i][1] = fmx(sp[2], sp[3]); t[i][2] = fmx(sc[0], sc[1]); t[i][3] = fmx(sc[2], sc[3]);
    m[i] = fmx(fmx(t[i][0], t[i][1]), fmx(t[i][2], t[i][3]));
  }
  bcast4(reduce4<true>(m[0], m[1], m[2], m[3]) * -c2, m[0], m[1], m[2], m[3]);
  bcast4(reduce4<true>(m[4], m[5], m[6], m[7]) * -c2, m[4], m[5], m[6], m[7]);
  float l[8];
#pragma unroll
  for (int i = 0; i < 8; ++i) {
#pragma unroll
    for (int r = 0; r < 4; ++r) t[i][r] = __builtin_amdgcn_exp2f(fmaf(t[i][r], c2, m[i]));
    l[i] = (t[i][0] + t[i][1]) + (t[i][2] + t[i][3]);
  }
  bcast4(__builtin_amdgcn_rcpf(reduce4<false>(l[0], l[1], l[2], l[3])), l[0], l[1], l[2], l[3]);
  bcast4(__builtin_amdgcn_rcpf(reduce4<false>(l[4], l[5], l[6], l[7])), l[4], l[5], l[6], l[7]);
  const int H = hg * 4 + W;
  const rsrc_t ro = out_rsrc(ws + A2_OFF, (unsigned)A2_BYTES);
  unsigned vm = (mt * 2 * KS2 + (H >> 1)) * 4096 + (H & 1) * 512 + (g & 1) * 1024 + (g >> 1) * 256 + c * 16;
  unsigned vx_ = (mt * 2 * KS2 + 16 + (hg >> 1)) * 4096 + (hg & 1) * 512 + g * 1024 + (W >> 1) * 256 + c * 16 + (W & 1) * 8;
  u32x4 pf[8];
#pragma unroll
  for (int i = 0; i < 8; ++i) {
    const float sc = l[i] * gt[i];
    const unsigned p01 = pack2(t[i][0] * sc, t[i][1] * sc), p23 = pack2(t[i][2] * sc, t[i][3] * sc);
    pf[i] = u32x4{__builtin_amdgcn_perm(p01, 0u, sel_lo), __builtin_amdgcn_perm(p01, 0u, sel_hi),
                  __builtin_amdgcn_perm(p23, 0u, sel_lo), __builtin_amdgcn_perm(p23, 0u, sel_hi)};
  }
  f32x4 om[8], ox[8];
#pragma unroll
  for (int i = 0; i < 8; ++i) {
    const int ip = i == 0 ? 8 : i - 1;
    const f32x4 z = {0.f, 0.f, 0.f, 0.f};
    om[i] = mfma16(cat(pk.v[ip], pk.v[i]), pf[i], z);
    ox[i] = mfma16(cat(vx[ip], vx[i]), pf[i], z);
  }
  SCHED();
  const bool g1 = g & 1, g2 = g & 2;
#pragma unroll
  for (int h = 0; h < 2; ++h) {
    u32x2 sm[4];
    unsigned ex[4], ey[4];
#pragma unroll
    for (int i = 0; i < 4; ++i) {
      sm[i] = u32x2{pack2(om[4 * h + i][0], om[4 * h + i][1]), pack2(om[4 * h + i][2], om[4 * h + i][3])};
      ex[i] = pack2(ox[4 * h + i][0], ox[4 * h + i][1]); ey[i] = pack2(ox[4 * h + i][2], ox[4 * h + i][3]);
    }
    bst16<ST_AUX>(ro, vm, widen_pair(sm[0], sm[1]));
    bst16<ST_AUX>(ro, vm + 2048, widen_pair(sm[2], sm[3]));
    unsigned x01 = g1 ? ex[1] : ex[0], x23 = g1 ? ex[3] : ex[2], y01 = g1 ? ey[1] : ey[0], y23 = g1 ? ey[3] : ey[2];
    OPAQUE(x01); OPAQUE(x23); OPAQUE(y01); OPAQUE(y23);
    bst8<ST_AUX>(ro, vx_, 0, u32x2{g2 ? x23 : x01, g2 ? y23 : y01});
    vm += KS2 * 4096; vx_ += KS2 * 4096;
  }
}

__device__ __forceinline__ unsigned mish2(float h0, float h1, float sc) {
  f32x2 h = {h0, h1};
  f32x2 a = h * 1.4426950408889634f;
  f32x2 w = {__builtin_amdgcn_exp2f(a.x), __builtin_amdgcn_exp2f(a.y)};
  w = w + 1.f;
  f32x2 d = __builtin_elementwise_fma(w, w, f32x2{1.f, 1.f});
  f32x2 r = {__builtin_amdgcn_rcpf(d.x), __builtin_amdgcn_rcpf(d.y)};
  f32x2 t = __builtin_elementwise_fma(r, f32x2{-2.f * sc, -2.f * sc}, f32x2{sc, sc});
  f32x2 y = h * t;
  return pack2(y.x, y.y);
}

typedef unsigned long long u64;
typedef __attribute__((address_space(1))) u64 gu64;
__device__ __forceinline__ u64 sy_ld(const gu64* p) { return __hip_atomic_load(p, __ATOMIC_RELAXED, __HIP_MEMORY_SCOPE_AGENT); }
__device__ __forceinline__ void sy_st(gu64* p, u64 v) { __hip_atomic_store(p, v, __ATOMIC_RELAXED, __HIP_MEMORY_SCOPE_AGENT); }

__device__ __forceinline__ void mlp_role(int lane, int wave, int mt, int nb0, const u32x2 (&bias)[7],
                                         unsigned char* __restrict__ ws, gu64* early, u64 epoch) {
  const int c = lane & 15, g = lane >> 4, q4 = (lane >> 2) & 3, p4 = lane & 3;
  const int wm = wave >> 1, wn = wave & 1;
  MlpOps o;
#pragma unroll
  for (int n = 0; n < 7; ++n) {
    f32x4 b = {bf_lo(bias[n].x), bf_hi(bias[n].x), bf_lo(bias[n].y), bf_hi(bias[n].y)};
#pragma unroll
    for (int r = 0; r < 4; ++r) o.a.c[r][n] = b;
  }
  o.xa = wm * 4 * 2048 + c * 128 + ((g ^ ((c >> 1) & 7)) * 16);
  o.wa = X_BYTES + ((g >> 1) * 16 + (g & 1) * 4 + q4) * W_ROW + wn * 8 * 32 + 8 * p4;
  o.d6 = wn ? (unsigned)-32 : 192u;
  STAMP(ws, 0, 1);
  k_loop(o);
  STAMP(ws, 0, 2);

  const rsrc_t ro = out_rsrc(ws + A2_OFF, (unsigned)A2_BYTES);
  const unsigned so = ((mt * 2 + wm) * KS2 + 20 + (nb0 >> 1)) * 4096;
  const unsigned vo = lane * 16;
  float sc[4];
#pragma unroll
  for (int r = 0; r < 4; ++r) sc[r] = 1.f - *(const LAS float*)(uintptr_t)(K1_G + (wm * 64 + r * 16 + c) * 4);
#pragma unroll
  for (int j = 0; j < 3; ++j) {
    unsigned vj = vo + so + (wn * 4 + j) * 4096;
    if (j == 2 && wn == 1 && nb0 >= 84) vj = 0x40000000u;
    OPAQUE(vj);
#pragma unroll
    for (int r = 0; r < 4; ++r) {
      const f32x4 h0 = o.a.c[r][2 * j], h1 = o.a.c[r][2 * j + 1];
      bst16<ST_AUX>(ro, vj + r * 1024,
                    u32x4{mish2(h0[0], h0[1], sc[r]), mish2(h0[2], h0[3], sc[r]), mish2(h1[0], h1[1], sc[r]), mish2(h1[2], h1[3], sc[r])});
    }
    SCHED();
  }
  WAIT_V(4);
  if (lane == 0) sy_st(early, epoch);
  const unsigned v6 = so + 3 * 4096 + wn * 512 + (g & 1) * 1024 + (g >> 1) * 256 + c * 16;
#pragma unroll
  for (int r = 0; r < 4; r += 2) {
    const f32x4 h0 = o.a.c[r][6], h1 = o.a.c[r + 1][6];
    bst16<ST_AUX>(ro, v6 + r * 1024, widen_pair(u32x2{mish2(h0[0], h0[1], sc[r]), mish2(h0[2], h0[3], sc[r])},
                                                u32x2{mish2(h1[0], h1[1], sc[r + 1]), mish2(h1[2], h1[3], sc[r + 1])}));
  }
}

__device__ __forceinline__ int w2cat_row(int beta, int rho) {
  if (beta < 32) return 20 * beta + rho;
  if (beta < 40) return 20 * (4 * (beta - 32) + (rho >> 2)) + 16 + (rho & 3);
  return 16 * (beta - 40) + rho;
}

__device__ __forceinline__ int kb_of(int t) { return 20 + 7 * t - (t == 7); }
__device__ __forceinline__ int ks_front(int w, int n) {
  if (n < 3) { const int e = 20 + 3 * w + n; return kb_of(e >> 2) + (e & 1) + 2 * (e & 2); }
  if (n < 7) return kb_of(2 * w + ((n - 3) >> 1)) + 2 + ((n - 3) & 1);
  return 4 + 4 * w + n - 7;
}
__device__ __forceinline__ int ks_back(int lw, int n) {
  if (n < 5) return kb_of(n) + (lw & 1) + 2 * (lw & 2);
  const int q = 4 * (n - 5) + lw;
  return q < 6 ? kb_of(q) + 6 : (q < 10 ? q - 6 : 0x40000);
}
constexpr int P2_REG = 7 * 5120;
static_assert(4 * P2_REG <= EX_OFF, "phase 2 re-uses the phase 1 ring");

typedef u32x4 __attribute__((aligned(8))) u32x4_a8;
#define POLL_SLEEP 4
__device__ __forceinline__ void flags_wait(const gu64* f, const gu64* bs) {
  const u64 want = sy_ld(bs) + 1;
#pragma unroll 1
  for (int i = 0; i < SPIN_CAP; ++i) { if (__all(sy_ld(f) == want)) break; __builtin_amdgcn_s_sleep(POLL_SLEEP); }
}
__device__ __forceinline__ bool fb_up(const gu64* sy, int lane) {
  bool ok = true;
#pragma unroll
  for (int i = 0; i < 4; ++i) ok &= sy_ld(sy + SY_FB + 64 * i + lane) == sy_ld(sy + SY_BASE + 64 * i + lane) + 1;
  return __all(ok);
}
#define ACQUIRE() __builtin_amdgcn_fence(__ATOMIC_ACQUIRE, "wavefront")
__device__ __forceinline__ void ld16_dev(u32x4& r, unsigned voff, u32x4 rsrc, unsigned soff) {
  asm volatile("buffer_load_dwordx4 %0, %1, %2, %3 offen sc1" : "=v"(r) : "v"(voff), "s"(rsrc), "s"(soff) : "memory");
}
__device__ __forceinline__ void ld8_dev(u64& r, const gu64* p) { asm volatile("global_load_dwordx2 %0, %1, off sc1" : "=v"(r) : "v"(p) : "memory"); }
__device__ __forceinline__ void ld8_buf(u64& r, unsigned voff, u32x4 rsrc, unsigned soff) {
  asm volatile("buffer_load_dwordx2 %0, %1, %2, %3 offen sc1" : "=v"(r) : "v"(voff), "s"(rsrc), "s"(soff) : "memory");
}
#define PIN(x) asm volatile("" : "+v"(x))
template <int N> __device__ __forceinline__ void wait_v() { asm volatile("s_waitcnt vmcnt(%0)" :: "i"(N) : "memory"); }

__device__ __forceinline__ void mma20(f32x4 (&acc)[4][5], const u32x4 (&a)[4], const u32x4 (&b)[5]) {
#pragma unroll
  for (int i = 0; i < 4; ++i)
#pragma unroll
    for (int j = 0; j < 5; ++j) asm volatile("v_mfma_f32_16x16x32_bf16 %0, %1, %2, %0" : "+v"(acc[i][j]) : "v"(b[j]), "v"(a[i]));
}
__device__ __forceinline__ void acc_zero(f32x4 (&acc)[4][5]) {
#pragma unroll
  for (int i = 0; i < 4; ++i)
#pragma unroll
    for (int j = 0; j < 5; ++j) { acc[i][j] = f32x4{0.f, 0.f, 0.f, 0.f}; OPAQUE(acc[i][j]); }
  asm volatile("s_nop 1");
}
#define ACC_OUT() do { SCHED(); asm volatile("s_nop 7\n\ts_nop 3"); SCHED(); } while (0)
template <int N> __device__ __forceinline__ void wait_pin(u32x4 (&a)[4]) {
  asm volatile("s_waitcnt vmcnt(%4)" : "+v"(a[0]), "+v"(a[1]), "+v"(a[2]), "+v"(a[3]) : "i"(N) : "memory");
}

struct Stream2 {
  u32x4 rs;
  unsigned va[4], vo, sa, sb;
  int lw;
  u32x4 fb[5][5], fa[4][4];
  f32x4 acc[4][5];
  template <int N> __device__ __forceinline__ void load_b() {
    const unsigned so = sb + ks_back(lw, N) * 5120;
#pragma unroll
    for (int j = 0; j < 5; ++j) ld16_dev(fb[N % 5][j], vo, rs, so + j * 1024);
  }
  template <int N> __device__ __forceinline__ void load_a() {
    const unsigned so = sa + ks_back(lw, N) * 4096;
#pragma unroll
    for (int i = 0; i < 4; ++i) ld16_dev(fa[N % 4][i], va[i], rs, so);
  }
  template <int N, int YOUNGER> __device__ __forceinline__ void step() {
    wait_pin<YOUNGER>(fa[N % 4]);
#pragma unroll
    for (int j = 0; j < 5; ++j) PIN(fb[N % 5][j]);
    mma20(acc, fa[N % 4], fb[N % 5]);
    SCHED();
  }
  __device__ __forceinline__ void run() {
    load_a<0>(); load_a<1>(); load_a<2>(); load_a<3>();
    step<0, 12>(); step<1, 8>();
    load_a<4>(); load_b<5>(); lds_wait(0, 2u); ACQUIRE(); load_a<5>(); load_b<6>();
    step<2, 22>(); step<3, 18>();
    load_b<7>(); lds_wait(0, 3u); ACQUIRE(); load_a<6>(); load_a<7>();
    step<4, 27>(); step<5, 18>(); step<6, 4>(); step<7, 0>();
    ACC_OUT();
  }
};

__global__ __launch_bounds__(512, 2) void fused_kernel(const unsigned short* __restrict__ x, const unsigned short* __restrict__ wqkv,
                                                       const unsigned short* __restrict__ wo, const unsigned short* __restrict__ w1,
                                                       const unsigned short* __restrict__ b1, const unsigned short* __restrict__ w2,
                                                       const unsigned short* __restrict__ gate_w, unsigned char* ws,
                                                       unsigned short* __restrict__ out) {
  extern __shared__ __attribute__((aligned(16))) unsigned char smem[];
  const int lane = threadIdx.x & 63;
  const int wave = __builtin_amdgcn_readfirstlane(threadIdx.x >> 6);
  const int b = blockIdx.x, xcd = b & 7, idx = b >> 3;
  const int mt = (xcd & 1) * 8 + (idx & 7);
  const int nsel = idx >> 3;
  const int nt = (xcd >> 1) * 2 + (nsel & 1);
  const bool attn = nsel < 2;
  const int m0 = mt * 128;
  const int nb0 = nt < 7 ? nt * 14 : 96;
  const int L = mt * 16 + (attn ? 0 : 8) + nt;
  const int mt2 = (xcd & 3) * 8 + (idx & 7);
  const int nt2 = (xcd >> 2) * 4 + (idx >> 3);
  gu64* sy = (gu64*)(ws + SY_OFF);
  STAMP(ws, 0, 0);
  if (idx == 16 && wave < 4) {
    uint64_t pc;
    asm volatile("s_getpc_b64 %0" : "=s"(pc));
    const u32x4 rc = make_rsrc((const void*)(pc & ~(uint64_t)255), CODE_BYTES);
    dma_touch(K1_PF, (unsigned)(wave * 64 + lane) * 128u, rc, 0);
    dma_touch(K1_PF, (unsigned)(wave * 64 + lane) * 128u, rc, 32768);
  }

  if (__builtin_expect(wave >= 4, 1)) {
    const int lw = wave - 4;
    u64 pf[4], pb[4];
    {
      Stager st;
      st.rx = make_rsrc(x, S_TOK * D * 2);
      st.vx = (unsigned)((8 * lw + (lane >> 3)) * (D * 2) + (((lane & 7) ^ ((4 * lw + (lane >> 4)) & 7)) * 16));
      st.sx = (unsigned)(m0 * D * 2);
      st.lx = lw * 1024;
      stage_x(st, 0);
      st.rw = make_rsrc(attn ? wqkv : w1, attn ? D * 3 * D * 2 : D * F * 2);
      const unsigned rowbytes = attn ? 3 * D * 2 : F * 2, secstride = attn ? D * 2 - 160 : 0;
      const int chmax = attn ? 29 : (nt < 6 ? 27 : 23);
      st.wstep = 64u * rowbytes;
      st.sw = attn ? nt * 160 : nb0 * 32;
      st.lw = X_BYTES + lw * 1024;
#pragma unroll
      for (int j = 0; j < 7; ++j) st.vw[j] = w_src(lw + 4 * j, lane, rowbytes, secstride, chmax);
      {
        unsigned v4w = w_src(26 + lw, lane, rowbytes, secstride, chmax);
        const bool hx = lw < 2;
        st.v4 = hx ? st.vx : v4w;
        st.r4 = hx ? st.rx : st.rw;
        st.s4 = hx ? (attn ? (m0 ? st.sx - 16 * D * 2 : 0u) : 0x40000000u) : st.sw;
        st.step4 = hx ? 128u : st.wstep;
        st.l4 = hx ? (16 + lw) * 1024 : X_BYTES + (26 + lw) * 1024;
      }
      stage_w(st, 0); stage_next(st);
      stage_x(st, STAGE); stage_w(st, STAGE); stage_next(st);
      const u64 epl = *(const __attribute__((address_space(4))) u64*)(uintptr_t)(ws + SY_OFF + (SY_BASE + L) * 8) + 1;
      WAIT_V(12);
      STAMP(ws, 0, 1);
      BAR();
      const int unit = lw < 2 ? lw * 256 + b : 384 + b;
      const bool tile = lw < 2 || (lw == 2 && b >= 128 && b < 208);
      const int uks = unit >> 3;
      const unsigned lt = EX_OFF + lw * 5120;
      if (tile) {
        const u32x4 r2 = make_rsrc(uks < 20 ? wo : w2, uks < 20 ? D * D * 2 : F * D * 2);
#pragma unroll
        for (int i = 0; i < 5; ++i) {
          unsigned ci = i * 64 + lane, row = (ci * 205u) >> 11, ch = ci - row * 10u;
          dma16(lt + i * 1024, (unsigned)w2cat_row(2 * uks + (row >> 4), row & 15) * (D * 2) + ch * 16, r2, (unit & 7) * 160);
        }
      }
      stage_x(st, 2 * STAGE); stage_w(st, 2 * STAGE); stage_next(st);
      unsigned sb = 0;
#pragma unroll 1
      for (int t = 0; t < NT1 - 3; ++t) {
        if (tile && (t == 0 || t == 2)) WAIT_V(17); else WAIT_V(12);
        BAR();
        if (t == 3 && lw == 0 && lane == 0) sy_st(sy + SY_FB + L, epl);
        if (t == 6 && lw == 0) {
#pragma unroll
          for (int i = 0; i < 4; ++i) { ld8_dev(pf[i], sy + SY_FB + 64 * i + lane); ld8_dev(pb[i], sy + SY_BASE + 64 * i + lane); }
        }
        stage_x(st, sb); stage_w(st, sb); stage_next(st);
        if (t == 1 && tile) {
          const bool half = uks < 20 || uks == 71 || (uks < 66 && (uks - 23) % 7 == 0);
          const unsigned ra = lt + ((half ? 8 : 4) * (lane >> 4) + ((lane >> 2) & 3)) * 160 + (lane & 3) * 40;
          const unsigned rb = ra + (half ? 4 : 16) * 160;
          unsigned char* bp = ws + B2_OFF + ((size_t)(unit & 7) * KS2 + uks) * 5120 + lane * 16;
#pragma unroll
          for (int i = 0; i < 5; ++i) st16(bp + i * 1024, cat(lds_tr(ra + 8 * i), lds_tr(rb + 8 * i)));
        }
        sb += STAGE; if (sb == NSTAGE * STAGE) sb = 0;
      }
    }
    Stream2 ld;
    ld.rs = make_rsrc(ws, (unsigned)(A2_BYTES + B2_BYTES + 8192));
    ld.vo = lane * 16;
#pragma unroll
    for (int i = 0; i < 4; ++i) ld.va[i] = lane * 16 + ((i + lw) & 3) * 1024;
    const unsigned sb0 = (unsigned)B2_OFF + nt2 * (KS2 * 5120);
    ld.sa = (unsigned)A2_OFF + mt2 * (KS2 * 4096); ld.sb = sb0; ld.lw = lw;
    WAIT_V(12);
    if (lw == 0) {
      bool ok = true;
#pragma unroll
      for (int i = 0; i < 4; ++i) {
        PIN(pf[i]); PIN(pb[i]); ok &= pf[i] == pb[i] + 1;
        *(LAS u64*)(uintptr_t)(K1_BASES + i * 512 + lane * 8) = pb[i];
      }
      if (!__all(ok)) {
#pragma unroll 1
        for (int i = 0; i < SPIN_CAP; ++i) { if (fb_up(sy, lane)) break; __builtin_amdgcn_s_sleep(POLL_SLEEP); }
      }
    }
    BAR();
    ACQUIRE();
    if (lw == 1 || lw == 2)
      asm volatile("s_mov_b32 m0, %0\n\ts_nop 0\n\tbuffer_load_dword %1, %2, %3 offen sc1 lds"
                   :: "s"(K1_G + (lw - 1) * 256), "v"(lane * 4), "s"(ld.rs), "s"((unsigned)G_OFF + (m0 + (lw - 1) * 64) * 4) : "memory");
    ld.load_b<0>(); ld.load_b<1>(); ld.load_b<2>();
    WAIT_V(15); BAR();
    ld.load_b<3>(); ld.load_b<4>();
    BAR();
    STAMP(ws, 0, 2);
    auto stage_b = [&](int n) {
      const int ks = ks_front(lw, n);
#pragma unroll
      for (int j = 0; j < 5; ++j) dma16_dev(lw * P2_REG + n * 5120 + j * 1024, ld.vo, ld.rs, sb0 + ks * 5120 + j * 1024);
    };
#define THIN 5
    stage_b(0); stage_b(1);
    wait_v<THIN>(); stage_b(2); wait_v<THIN>(); stage_b(3); wait_v<THIN>(); stage_b(4); wait_v<THIN>();
    const int Lp = (mt2 >> 1) * 16 + 8 + ((lane >> 1) & 7);
    const gu64* fm = sy + SY_FE + Lp * 4 + (mt2 & 1) * 2 + (lane & 1);
    stage_b(5); wait_v<THIN>(); stage_b(6);
    if (lw == 0) { ld8_dev(pf[0], fm); ld8_dev(pb[0], sy + SY_BASE + Lp); }
    acc_zero(ld.acc);
    WAIT_V(0);
    lds_post(4 + lw, 1u, lane);
    STAMP(ws, 0, 3);
    if (lw == 0) {
      PIN(pf[0]); PIN(pb[0]);
#pragma unroll 1
      for (int k = 0; k < SPIN_CAP && !__all(pf[0] == pb[0] + 1); ++k) { __builtin_amdgcn_s_sleep(POLL_SLEEP); pf[0] = sy_ld(fm); }
      lds_post(0, 1u, lane);
    } else lds_wait(0, 1u);
    ACQUIRE();
    STAMP(ws, 0, 4);
    ld.run();
    STAMP(ws, 0, 5);
    lds_wait(8 + lw, 1u);
    unsigned rr = lw * P2_REG + lane * 16;
    OPAQUE(rr);
#pragma unroll
    for (int i = 0; i < 4; ++i)
#pragma unroll
      for (int j = 0; j < 5; ++j)
        if (i > 0 || j < 2) *(LAS f32x4*)(uintptr_t)(rr + (i == 0 ? 18 + j : 15 + i * 5 + j) * 1024) = ld.acc[i][j];
    WAIT_L0();
    BAR();
    f32x4 r[3];
#pragma unroll
    for (int j = 2; j < 5; ++j) r[j - 2] = ld.acc[0][j] + *(const LAS f32x4*)(uintptr_t)(rr + (j - 2) * 1024);
#pragma unroll
    for (int d = 1; d < 4; ++d) {
      unsigned rq = ((lw - d) & 3) * P2_REG + lane * 16;
      OPAQUE(rq);
#pragma unroll
      for (int j = 2; j < 5; ++j)
        r[j - 2] += *(const LAS f32x4*)(uintptr_t)(rq + (d * 5 + j - 2) * 1024) + *(const LAS f32x4*)(uintptr_t)(rq + (15 + d * 5 + j) * 1024);
    }
    unsigned* op = (unsigned*)(out + (size_t)(mt2 * 64 + lw * 16 + (lane & 15)) * D + nt2 * 80 + (lane >> 4) * 20);
    *(u32x4_a8*)(op + 4) = u32x4{pack2(r[0][0], r[0][1]), pack2(r[0][2], r[0][3]), pack2(r[1][0], r[1][1]), pack2(r[1][2], r[1][3])};
    *(u32x2*)(op + 8) = u32x2{pack2(r[2][0], r[2][1]), pack2(r[2][2], r[2][3])};
    STAMP(ws, 0, 6);
    return;
  }

  const u64 epoch = *(const __attribute__((address_space(4))) u64*)(uintptr_t)(ws + SY_OFF + (SY_BASE + L) * 8) + 1;
  if (threadIdx.x < 16) *(LAS unsigned*)(uintptr_t)(K1_NEWS + threadIdx.x * 4) = 0u;
  u32x2 bias[7];
  if (!attn) {
    const u32x2* bp = (const u32x2*)(b1 + (lane >> 4) * 4);
#pragma unroll
    for (int n = 0; n < 7; ++n) bias[n] = bp[min(nb0 + (n < 6 ? (wave & 1) * 8 + n : 6 + (wave & 1)), 107) * 4];
  }
  {
    const int tok = L * 8 + wave * 2 + (lane >> 5);
    const u32x2* gp = (const u32x2*)gate_w + (lane & 31);
    const u32x2* xr = (const u32x2*)(x + (size_t)tok * D) + (lane & 31);
    float sg = 0.f;
#pragma unroll
    for (int j = 0; j < 5; ++j) {
      const u32x2 a = xr[32 * j], w = gp[32 * j];
      sg = fmaf(bf_lo(a.x), bf_lo(w.x), sg); sg = fmaf(bf_hi(a.x), bf_hi(w.x), sg);
      sg = fmaf(bf_lo(a.y), bf_lo(w.y), sg); sg = fmaf(bf_hi(a.y), bf_hi(w.y), sg);
    }
#pragma unroll
    for (int o = 16; o >= 1; o >>= 1) sg += __shfl_xor(sg, o, 64);
    if ((lane & 31) == 0)
      __hip_atomic_store((__attribute__((address_space(1))) float*)(ws + G_OFF) + tok, 1.f / (1.f + __expf(-sg)),
                         __ATOMIC_RELAXED, __HIP_MEMORY_SCOPE_AGENT);
  }
  if (!attn) {
#pragma unroll
    for (int n = 0; n < 7; ++n) PIN(bias[n]);
  }
#define PF_ATTN 0
  if (attn == (PF_ATTN != 0)) {
    const int wg = b & 127;
    const int pw = wg * 4 + wave;
    const int li = pw * 28 + (lane < 28 ? lane : 0);
    dma_touch(K1_PF, (unsigned)((li / 7) * (D * 2) + (3 + li % 7) * 128), make_rsrc(x, S_TOK * D * 2), 0);
    {
      const int q = ((idx & 15) * 4 + wave) * 63 + (lane < 63 ? lane : 0), row = q / 9, sl = q - row * 9;
      dma_touch(K1_PF, (unsigned)(row * (3 * D * 2) + (sl / 3) * (D * 2) + (sl % 3) * 128 + (xcd >> 1) * 320), make_rsrc(wqkv, D * 3 * D * 2), 192 * 3 * D * 2);
    }
    dma_touch(K1_PF, (unsigned)((pw * 24 + (lane < 24 ? lane : 0)) * 128), make_rsrc(w1, D * F * 2), 192 * F * 2);
  }
  WAIT_L0();
  if (attn) {
    AttnPacked pk;
    if (wave == 0) attn_main<0>(lane, pk, ws);
    else if (wave == 1) attn_main<1>(lane, pk, ws);
    else if (wave == 2) attn_main<2>(lane, pk, ws);
    else attn_main<3>(lane, pk, ws);
    attn_epilogue(lane, wave, mt, nt, pk, ws);
  } else {
    mlp_role(lane, wave, mt, nb0, bias, ws, sy + SY_FE + L * 4 + wave, epoch);
  }
  WAIT_V(0);
  if (lane == 0) sy_st(sy + SY_FA + L * 4 + wave, epoch);
  STAMP(ws, 0, 3);

  int ln = lane;
  OPAQUE(ln);
  const u32x4 rs = make_rsrc(ws, (unsigned)SY_OFF + 32768);
  const unsigned sa = (unsigned)A2_OFF + mt2 * (KS2 * 4096);
  unsigned va[4];
#pragma unroll
  for (int i = 0; i < 4; ++i) va[i] = ln * 16 + ((i + wave) & 3) * 1024;
  u32x4 fa[7][4], ea[4][4], eb[4][5];
  const int La = (mt2 >> 1) * 16 + (ln < 32 ? (ln >> 2) : 8 + ((ln >> 1) & 7));
  const gu64* fat = sy + SY_FA + La * 4 + (ln < 32 ? (ln & 3) : (mt2 & 1) * 2 + (ln & 1));
  u64 pp[4];
  u64 up = 0;
  f32x4 acc[4][5];
  acc_zero(acc);
  unsigned rb = wave * P2_REG + ln * 16;
  OPAQUE(rb);
  const unsigned sbb = (unsigned)B2_OFF + nt2 * (KS2 * 5120), vo = ln * 16;
#define P2_STEP(n, CNT) { u32x4 fb[5]; _Pragma("unroll") for (int j = 0; j < 5; ++j) fb[j] = lds_read128(rb + n * 5120 + j * 1024); \
    wait_pin<CNT>(fa[n]); mma20(acc, fa[n], fb); SCHED(); }
#define P2_EXTRA_B(e) { _Pragma("unroll") for (int j = 0; j < 5; ++j) ld16_dev(eb[e][j], vo, rs, sbb + ks_front(wave, 7 + e) * 5120 + j * 1024); }
#define P2_EXTRA_A(e) { _Pragma("unroll") for (int i = 0; i < 4; ++i) ld16_dev(ea[e][i], va[i], rs, sa + ks_front(wave, 7 + e) * 4096); }
  P2_EXTRA_B(0) P2_EXTRA_B(1)
  lds_wait(4 + wave, 1u);
  lds_wait(0, 1u);
  ACQUIRE();
  if (wave == 0) {
    ld8_dev(pp[0], fat); ld8_dev(pp[1], sy + SY_BASE + La);
  }
#pragma unroll
  for (int n = 0; n < 3; ++n)
#pragma unroll
    for (int i = 0; i < 4; ++i) ld16_dev(fa[n][i], va[i], rs, sa + ks_front(wave, n) * 4096);
  if (wave == 0) {
    WAIT_V(12);
    PIN(pp[0]); PIN(pp[1]);
    up = __ballot(pp[0] == pp[1] + 1);
#pragma unroll 1
    for (int k = 0; k < SPIN_CAP && (up >> 32) != 0xffffffffull; ++k) { __builtin_amdgcn_s_sleep(POLL_SLEEP); up = __ballot(sy_ld(fat) == pp[1] + 1); }
    lds_post(0, up == ~0ull ? 3u : 2u, ln);
  }
  STAMP(ws, 0, 4);
  P2_STEP(0, 8)
  lds_wait(0, 2u);
  ACQUIRE();
  if (wave == 0) ld8_dev(pp[0], fat);
#pragma unroll
  for (int n = 3; n < 7; ++n)
#pragma unroll
    for (int i = 0; i < 4; ++i) ld16_dev(fa[n][i], va[i], rs, sa + ks_front(wave, n) * 4096);
  P2_STEP(1, 20)
  P2_STEP(2, 16)
  if (wave == 0) {
    PIN(pp[0]);
    up |= __ballot(pp[0] == pp[1] + 1);
#pragma unroll 1
    for (int k = 0; k < SPIN_CAP && up != ~0ull; ++k) { up = __ballot(sy_ld(fat) == pp[1] + 1); if (up != ~0ull) __builtin_amdgcn_s_sleep(POLL_SLEEP); }
    lds_post(0, 3u, ln);
    if (ln == 0) sy_st(sy + SY_PASS + L, epoch);
  }
  P2_EXTRA_B(2)
  P2_STEP(3, 17) P2_STEP(4, 13)
  lds_wait(0, 3u);
  ACQUIRE();
  P2_EXTRA_A(0) P2_EXTRA_A(1)
  P2_STEP(5, 17)
  P2_EXTRA_A(2)
  P2_STEP(6, 17)
  P2_EXTRA_B(3) P2_EXTRA_A(3)
#define P2_EXTRA(e, CNT) { wait_pin<CNT>(ea[e]); _Pragma("unroll") for (int j = 0; j < 5; ++j) PIN(eb[e][j]); mma20(acc, ea[e], eb[e]); SCHED(); }
  P2_EXTRA(0, 17) P2_EXTRA(1, 13) P2_EXTRA(2, 9) P2_EXTRA(3, 0)
#undef P2_EXTRA
  ACC_OUT();
#undef P2_STEP
#undef P2_EXTRA_A
#undef P2_EXTRA_B
  if (wave == 0) {
#pragma unroll
    for (int i = 0; i < 4; ++i) ld8_buf(pp[i], ln * 8, rs, (unsigned)SY_OFF + (SY_PASS + 64 * i) * 8);
  }
#pragma unroll
  for (int i = 0; i < 4; ++i)
#pragma unroll
    for (int j = 0; j < 5; ++j)
      if (i > 0 || j >= 2) *(LAS f32x4*)(uintptr_t)(rb + (i * 5 + j - 2) * 1024) = acc[i][j];
  WAIT_L0();
  lds_post(8 + wave, 1u, ln);
  STAMP(ws, 0, 5);
  BAR();
  {
    f32x4 r[2];
#pragma unroll
    for (int j = 0; j < 2; ++j) r[j] = acc[0][j] + *(const LAS f32x4*)(uintptr_t)(rb + (18 + j) * 1024);
#pragma unroll
    for (int d = 1; d < 4; ++d) {
      unsigned rq = ((wave - d) & 3) * P2_REG + ln * 16;
      OPAQUE(rq);
#pragma unroll
      for (int j = 0; j < 2; ++j)
        r[j] += *(const LAS f32x4*)(uintptr_t)(rq + (d * 5 + j - 2) * 1024) + *(const LAS f32x4*)(uintptr_t)(rq + (15 + d * 5 + j) * 1024);
    }
    if (wave == 0) WAIT_V(0);
    unsigned* op = (unsigned*)(out + (size_t)(mt2 * 64 + wave * 16 + (ln & 15)) * D + nt2 * 80 + (ln >> 4) * 20);
    *(u32x4_a8*)op = u32x4{pack2(r[0][0], r[0][1]), pack2(r[0][2], r[0][3]), pack2(r[1][0], r[1][1]), pack2(r[1][2], r[1][3])};
  }
  if (wave == 0) {
    u64 wb[4];
#pragma unroll
    for (int i = 0; i < 4; ++i) { PIN(pp[i]); wb[i] = *(const LAS u64*)(uintptr_t)(K1_BASES + i * 512 + ln * 8); }
#pragma unroll 1
    for (int k = 0; k < SPIN_CAP; ++k) {
      if (__all(pp[0] == wb[0] + 1 && pp[1] == wb[1] + 1 && pp[2] == wb[2] + 1 && pp[3] == wb[3] + 1)) break;
      __builtin_amdgcn_s_sleep(POLL_SLEEP);
#pragma unroll
      for (int i = 0; i < 4; ++i) pp[i] = sy_ld(sy + SY_PASS + 64 * i + ln);
    }
    if (ln == 0) sy_st(sy + SY_BASE + L, epoch);
  }
  STAMP(ws, 0, 6);
}

}

extern "C" void kernel_launch(void* const* d_in, const int* in_sizes, int n_in, void* d_out, int out_size, void* d_ws,
                              size_t ws_size, hipStream_t stream) {
  const unsigned short* x = (const unsigned short*)d_in[0];
  const unsigned short* wqkv = (const unsigned short*)d_in[1];
  const unsigned short* wo = (const unsigned short*)d_in[2];
  const unsigned short* w1 = (const unsigned short*)d_in[4];
  const unsigned short* b1 = (const unsigned short*)d_in[5];
  const unsigned short* w2 = (const unsigned short*)d_in[6];
  const unsigned short* gate_w = (const unsigned short*)d_in[7];
  (void)hipFuncSetAttribute((const void*)fused_kernel, hipFuncAttributeMaxDynamicSharedMemorySize, K1_LDS);
  fused_kernel<<<256, 512, K1_LDS, stream>>>(x, wqkv, wo, w1, b1, w2, gate_w, (unsigned char*)d_ws, (unsigned short*)d_out);
}
```

```cpp
#include <hip/hip_runtime.h>
#include <hip/hip_bf16.h>
#include <stdint.h>

#define STAMP(ws, kern, i)
#define CODE_BYTES 34816

namespace {

typedef __attribute__((ext_vector_type(4))) short s16x4;
typedef __attribute__((ext_vector_type(8))) __bf16 bf16x8;
typedef __attribute__((ext_vector_type(2))) __bf16 bf16x2;
typedef __attribute__((ext_vector_type(4))) float f32x4;
typedef __attribute__((ext_vector_type(2))) float f32x2;
typedef __attribute__((ext_vector_type(4))) unsigned u32x4;
typedef __attribute__((ext_vector_type(2))) unsigned u32x2;
#define LAS __attribute__((address_space(3)))

constexpr int S_TOK = 2048, D = 640, F = 1728;
constexpr int KS2 = 74;
constexpr size_t A2_BYTES = (size_t)128 * KS2 * 1024;
constexpr size_t B2_BYTES = (size_t)40 * KS2 * 1024;
constexpr size_t A2_OFF = 0, B2_OFF = A2_OFF + A2_BYTES, G_OFF = B2_OFF + B2_BYTES;
constexpr size_t SY_OFF = G_OFF + 8192;
constexpr int SY_BASE = 0, SY_PASS = 256, SY_FB = 512, SY_FA = 1024, SY_FE = 2048;

constexpr int X_BYTES = 144 * 128;
constexpr int W_ROW = 480;
constexpr int W_BYTES = 64 * W_ROW;
constexpr int STAGE = X_BYTES + W_BYTES;
constexpr int NSTAGE = 3;
constexpr int EX_OFF = NSTAGE * STAGE;
constexpr int EX_Q = EX_OFF, EX_K = EX_Q + 8 * 512, EX_V = EX_K + 9 * 512;
constexpr int K1_PF = EX_OFF + 3 * 5120;
constexpr int K1_BASES = EX_V + 9 * 512;
static_assert(K1_BASES + 2048 <= K1_PF, "LDS map");
constexpr int K1_NEWS = K1_PF + 256;
constexpr int K1_G = K1_NEWS + 64;
constexpr int K1_LDS = K1_G + 512;
constexpr int NT1 = 10;

__device__ __forceinline__ unsigned pack2(float a, float b) {
  f32x2 v = {a, b};
  bf16x2 r = __builtin_convertvector(v, bf16x2);
  return __builtin_bit_cast(unsigned, r);
}
__device__ __forceinline__ float bf_lo(unsigned u) { return __builtin_bit_cast(float, u << 16); }
__device__ __forceinline__ float bf_hi(unsigned u) { return __builtin_bit_cast(float, u & 0xffff0000u); }
__device__ __forceinline__ f32x4 mfma16(u32x4 a, u32x4 b, f32x4 c) {
  return __builtin_amdgcn_mfma_f32_16x16x32_bf16(__builtin_bit_cast(bf16x8, a), __builtin_bit_cast(bf16x8, b), c, 0, 0, 0);
}
__device__ __forceinline__ u32x4 lds_read128(unsigned addr) { return *(const LAS u32x4*)(uintptr_t)addr; }
__device__ __forceinline__ u32x2 lds_read64(unsigned addr) { return *(const LAS u32x2*)(uintptr_t)addr; }
__device__ __forceinline__ u32x2 lds_tr(unsigned addr) {
  s16x4 v = __builtin_amdgcn_ds_read_tr16_b64_v4i16((LAS s16x4*)(uintptr_t)addr);
  return __builtin_bit_cast(u32x2, v);
}
__device__ __forceinline__ void dma16(unsigned ldsaddr, unsigned voff, u32x4 rsrc, unsigned soff) {
  asm volatile("s_mov_b32 m0, %0\n\ts_nop 0\n\tbuffer_load_dwordx4 %1, %2, %3 offen lds"
               :: "s"(ldsaddr), "v"(voff), "s"(rsrc), "s"(soff) : "memory");
}
__device__ __forceinline__ void dma16_dev(unsigned ldsaddr, unsigned voff, u32x4 rsrc, unsigned soff) {
  asm volatile("s_mov_b32 m0, %0\n\ts_nop 0\n\tbuffer_load_dwordx4 %1, %2, %3 offen sc1 lds"
               :: "s"(ldsaddr), "v"(voff), "s"(rsrc), "s"(soff) : "memory");
}
__device__ __forceinline__ void dma_touch(unsigned ldsaddr, unsigned voff, u32x4 rsrc, unsigned soff) {
  asm volatile("s_mov_b32 m0, %0\n\ts_nop 0\n\tbuffer_load_dword %1, %2, %3 offen lds"
               :: "s"(ldsaddr), "v"(voff), "s"(rsrc), "s"(soff) : "memory");
}
__device__ __forceinline__ void st16(void* p, u32x4 v) {
  asm volatile("global_store_dwordx4 %0, %1, off sc1\n\ts_nop 1" :: "v"(p), "v"(v) : "memory");
}
constexpr int ST_AUX = 16;
typedef __amdgpu_buffer_rsrc_t rsrc_t;
__device__ __forceinline__ rsrc_t out_rsrc(void* p, unsigned bytes) { return __builtin_amdgcn_make_buffer_rsrc(p, 0, bytes, 0x00020000); }
template <int AUX> __device__ __forceinline__ void bst16(rsrc_t r, unsigned voff, u32x4 v) { __builtin_amdgcn_raw_buffer_store_b128(v, r, voff, 0, AUX); }
template <int AUX> __device__ __forceinline__ void bst8(rsrc_t r, unsigned voff, unsigned soff, u32x2 v) { __builtin_amdgcn_raw_buffer_store_b64(v, r, voff, soff, AUX); }
#define WAIT_V(n) asm volatile("s_waitcnt vmcnt(" #n ")" ::: "memory")
#define WAIT_L0() __builtin_amdgcn_s_waitcnt(0xC07F)
#define OPAQUE(x) asm volatile("" : "+v"(x))
#define BAR() do { asm volatile("" ::: "memory"); __builtin_amdgcn_s_barrier(); asm volatile("" ::: "memory"); } while (0)
#define SCHED() __builtin_amdgcn_sched_barrier(0)

__device__ __forceinline__ u32x4 make_rsrc(const void* p, unsigned bytes) {
  uint64_t a = (uint64_t)p;
  u32x4 r;
  r.x = __builtin_amdgcn_readfirstlane((unsigned)a);
  r.y = __builtin_amdgcn_readfirstlane((unsigned)(a >> 32) & 0xffffu);
  r.z = bytes;
  r.w = 0x00020000u;
  return r;
}

__device__ __forceinline__ int swap23(int k) { return (k & ~12) | ((k & 4) << 1) | ((k & 8) >> 1); }

struct Stager {
  unsigned vx, vw[7], v4;
  u32x4 rx, rw, r4;
  unsigned sx, sw, s4;
  unsigned wstep, step4;
  unsigned lx, lw, l4;
};
__device__ __forceinline__ void stage_x(const Stager& st, unsigned sb) {
#pragma unroll
  for (int p = 0; p < 4; ++p) dma16(sb + st.lx + p * 4096, st.vx, st.rx, st.sx + p * (32 * D * 2));
}
__device__ __forceinline__ void stage_w(const Stager& st, unsigned sb) {
#pragma unroll
  for (int j = 0; j < 7; ++j) dma16(sb + st.lw + j * 4096, st.vw[j], st.rw, st.sw);
  dma16(sb + st.l4, st.v4, st.r4, st.s4);
}
__device__ __forceinline__ void stage_next(Stager& st) { st.sx += 128u; st.sw += st.wstep; st.s4 += st.step4; }
__device__ __forceinline__ unsigned w_src(int wi, int lane, unsigned rowbytes, unsigned secstride, int chmax) {
  unsigned ci = wi * 64 + lane;
  unsigned pos = (ci * 2185u) >> 16, ch = ci - pos * 30u;
  unsigned sec = (ch * 205u) >> 11;
  unsigned k = swap23(pos);
  ch = ch < (unsigned)chmax ? ch : (unsigned)chmax;
  return k * rowbytes + ch * 16u + sec * secstride;
}

struct AttnAcc { f32x4 q[8], k[9], v[9], e[9]; };
__device__ __forceinline__ u32x4 cat(u32x2 a, u32x2 b) { return u32x4{a.x, a.y, b.x, b.y}; }

template <int W>
struct AttnOps {
  struct Frags { u32x4 x[9]; u32x2 wl[4], wh[4]; };
  AttnAcc a;
  unsigned xa, wa, we;
  static constexpr int per_rb(int rb) { return 2 + (rb < 8 ? 1 : 0) + ((W == 3 || (W == 0 && rb == 8)) ? 0 : 1); }
  static constexpr int NM = per_rb(8) + 8 * per_rb(0);
  static constexpr int NR = W < 3 ? 17 : 15;
  template <int I> __device__ __forceinline__ void mma(const Frags& f) {
    constexpr int rb = I < per_rb(8) ? 8 : (I - per_rb(8)) / per_rb(0);
    constexpr int j = I < per_rb(8) ? I : (I - per_rb(8)) % per_rb(0);
    constexpr bool has_e = !(W == 3 || (W == 0 && rb == 8));
    if constexpr (j == 0) a.k[rb] = mfma16(cat(f.wl[1], f.wh[1]), f.x[rb], a.k[rb]);
    else if constexpr (j == 1) a.v[rb] = mfma16(f.x[rb], cat(f.wl[2], f.wh[2]), a.v[rb]);
    else if constexpr (j == 2 && has_e) {
      if constexpr (W == 2) a.e[rb] = mfma16(f.x[rb], cat(f.wl[3], f.wh[3]), a.e[rb]);
      else a.e[rb] = mfma16(cat(f.wl[3], f.wh[3]), f.x[rb], a.e[rb]);
    } else a.q[rb] = mfma16(cat(f.wl[0], f.wh[0]), f.x[rb], a.q[rb]);
  }
  unsigned x, w, e;
  __device__ __forceinline__ void bases(unsigned sb, int kk) {
    x = (xa ^ (kk * 64)) + sb; w = wa + sb + kk * 32 * W_ROW; e = we + sb + kk * 32 * W_ROW;
    OPAQUE(x); OPAQUE(w); if constexpr (W < 3) OPAQUE(e);
  }
  template <int I0> __device__ __forceinline__ void read(Frags& f) {
    constexpr int I = (W == 3 && I0 >= 5) ? I0 + 2 : I0;
    if constexpr (I == 0) f.wl[1] = lds_tr(w + 160);
    if constexpr (I == 1) f.wh[1] = lds_tr(w + 160 + 8 * W_ROW);
    if constexpr (I == 2) f.x[8] = lds_read128(x + 8 * 2048);
    if constexpr (I == 3) f.wl[2] = lds_tr(w + 320);
    if constexpr (I == 4) f.wh[2] = lds_tr(w + 320 + 8 * W_ROW);
    if constexpr (I == 5) f.wl[3] = lds_tr(e);
    if constexpr (I == 6) f.wh[3] = lds_tr(e + 8 * W_ROW);
    if constexpr (I == 7) f.x[0] = lds_read128(x);
    if constexpr (I == 8) f.wl[0] = lds_tr(w);
    if constexpr (I == 9) f.wh[0] = lds_tr(w + 8 * W_ROW);
    if constexpr (I >= 10) f.x[I - 9] = lds_read128(x + (I - 9) * 2048);
  }
};

struct MlpAcc { f32x4 c[4][7]; };
struct MlpOps {
  struct Frags { u32x4 x[4]; u32x2 wl[7], wh[7]; };
  MlpAcc a;
  unsigned xa, wa;
  static constexpr int NM = 28, NR = 18;
  template <int I> __device__ __forceinline__ void mma(const Frags& f) {
    constexpr int n = I / 4, r = I % 4;
    a.c[r][n] = mfma16(cat(f.wl[n], f.wh[n]), f.x[r], a.c[r][n]);
  }
  unsigned x, w, w6, d6;
  __device__ __forceinline__ void bases(unsigned sb, int kk) {
    x = (xa ^ (kk * 64)) + sb; w = wa + sb + kk * 32 * W_ROW; w6 = w + d6;
    OPAQUE(x); OPAQUE(w); OPAQUE(w6);
  }
  template <int I> __device__ __forceinline__ void read(Frags& f) {
    if constexpr (I < 2) { if constexpr (I == 0) f.wl[0] = lds_tr(w); else f.wh[0] = lds_tr(w + 8 * W_ROW); }
    else if constexpr (I < 6) f.x[I - 2] = lds_read128(x + (I - 2) * 2048);
    else {
      constexpr int n = 1 + (I - 6) / 2;
      const unsigned a = n < 6 ? w + 32 * n : w6;
      if constexpr ((I & 1) == 0) f.wl[n] = lds_tr(a); else f.wh[n] = lds_tr(a + 8 * W_ROW);
    }
  }
};

template <class OPS, int I>
__device__ __forceinline__ void read_all(OPS& o, typename OPS::Frags& f) {
  if constexpr (I < OPS::NR) { o.template read<I>(f); read_all<OPS, I + 1>(o, f); }
}
template <class OPS, int I>
__device__ __forceinline__ void half_step(OPS& o, const typename OPS::Frags& fc, typename OPS::Frags& fn) {
  if constexpr (I < OPS::NM) {
    o.template mma<I>(fc);
    constexpr int span = OPS::NM - 6;
    static_assert(span >= OPS::NR, "one read per gap");
    constexpr int j = (I * OPS::NR + span - 1) / span;
    if constexpr (j < OPS::NR && j * span / OPS::NR == I) { SCHED(); o.template read<j>(fn); }
    SCHED();
    half_step<OPS, I + 1>(o, fc, fn);
  }
}

template <class OPS>
__device__ __forceinline__ void k_loop(OPS& o) {
  typename OPS::Frags f0, f1;
  o.bases(0, 0);
  BAR();
  read_all<OPS, 0>(o, f0);
  unsigned sb = 0;
#pragma unroll 1
  for (int t = 0; t < NT1; ++t) {
    unsigned sn = sb + STAGE; if (sn == NSTAGE * STAGE) sn = 0;
    o.bases(sb, 1);
    WAIT_L0();
    SCHED();
    half_step<OPS, 0>(o, f0, f1);
    o.bases(sn, 0);
    WAIT_L0();
    if (t == 2) WAIT_V(0);
    BAR();
    SCHED();
    half_step<OPS, 0>(o, f1, f0);
    sb = sn;
  }
}

__device__ __forceinline__ float fmx(float a, float b) { return __builtin_elementwise_maximum(a, b); }
__device__ __forceinline__ void swap32(float& a, float& b) {
  auto r = __builtin_amdgcn_permlane32_swap(__builtin_bit_cast(unsigned, a), __builtin_bit_cast(unsigned, b), false, false);
  unsigned r0 = r[0], r1 = r[1];
  a = __builtin_bit_cast(float, r0); b = __builtin_bit_cast(float, r1);
}
__device__ __forceinline__ void swap16(float& a, float& b) {
  auto r = __builtin_amdgcn_permlane16_swap(__builtin_bit_cast(unsigned, a), __builtin_bit_cast(unsigned, b), false, false);
  unsigned r0 = r[0], r1 = r[1];
  a = __builtin_bit_cast(float, r0); b = __builtin_bit_cast(float, r1);
}
__device__ __forceinline__ u32x4 widen_pair(u32x2 a, u32x2 b) {
  auto x = __builtin_amdgcn_permlane16_swap(a.x, b.x, false, false);
  auto y = __builtin_amdgcn_permlane16_swap(a.y, b.y, false, false);
  unsigned x0 = x[0], x1 = x[1], y0 = y[0], y1 = y[1];
  return u32x4{x0, y0, x1, y1};
}
template <bool MAX>
__device__ __forceinline__ float reduce4(float x0, float x1, float x2, float x3) {
  swap32(x0, x1); swap32(x2, x3);
  float y01 = MAX ? fmx(x0, x1) : x0 + x1, y23 = MAX ? fmx(x2, x3) : x2 + x3;
  swap16(y01, y23);
  return MAX ? fmx(y01, y23) : y01 + y23;
}
__device__ __forceinline__ void bcast4(float z, float& x0, float& x1, float& x2, float& x3) {
  float u = z, w = z;
  swap16(u, w);
  x0 = u; x1 = u; swap32(x0, x1);
  x2 = w; x3 = w; swap32(x2, x3);
}

constexpr int SPIN_CAP = 1 << 16;
__device__ __forceinline__ void lds_wait(int word, unsigned atleast) {
  volatile LAS unsigned* p = (volatile LAS unsigned*)(uintptr_t)(K1_NEWS + word * 4);
#pragma unroll 1
  for (int i = 0; i < 16 * SPIN_CAP; ++i) { if (__builtin_amdgcn_readfirstlane(*p) >= atleast) break; __builtin_amdgcn_s_sleep(1); }
}
__device__ __forceinline__ void lds_post(int word, unsigned v, int lane) {
  if (lane == 0) *(volatile LAS unsigned*)(uintptr_t)(K1_NEWS + word * 4) = v;
}

struct AttnPacked { u32x2 q[8], k[9], v[9]; };

template <int W>
__device__ __forceinline__ void attn_main(int lane, AttnPacked& pk, unsigned char* __restrict__ ws) {
  OPAQUE(lane);
  const int c = lane & 15, g = lane >> 4, q4 = (lane >> 2) & 3, p4 = lane & 3;
  AttnOps<W> o;
#pragma unroll
  for (int i = 0; i < 9; ++i) {
    if (i < 8) o.a.q[i] = f32x4{0.f, 0.f, 0.f, 0.f};
    o.a.k[i] = f32x4{0.f, 0.f, 0.f, 0.f}; o.a.v[i] = f32x4{0.f, 0.f, 0.f, 0.f}; o.a.e[i] = f32x4{0.f, 0.f, 0.f, 0.f};
  }
  o.xa = c * 128 + ((g ^ ((c >> 1) & 7)) * 16);
  const unsigned wrow = X_BYTES + ((g >> 1) * 16 + (g & 1) * 4 + q4) * W_ROW;
  o.wa = wrow + 40 * W + 8 * p4;
  o.we = wrow + 160 * (W < 3 ? W : 0) + 40 * p4 + 32;
  STAMP(ws, 0, 1);
  k_loop(o);
  STAMP(ws, 0, 2);
  OPAQUE(lane);
  if constexpr (W < 3) {
    constexpr int base = W == 0 ? EX_Q : (W == 1 ? EX_K : EX_V);
#pragma unroll
    for (int i = 0; i < (W == 0 ? 8 : 9); ++i) {
      u32x2 v = {pack2(o.a.e[i][0], o.a.e[i][1]), pack2(o.a.e[i][2], o.a.e[i][3])};
      *(LAS u32x2*)(uintptr_t)(base + i * 512 + lane * 8) = v;
    }
  }
#pragma unroll
  for (int i = 0; i < 9; ++i) {
    if (i < 8) pk.q[i] = u32x2{pack2(o.a.q[i][0], o.a.q[i][1]), pack2(o.a.q[i][2], o.a.q[i][3])};
    pk.k[i] = u32x2{pack2(o.a.k[i][0], o.a.k[i][1]), pack2(o.a.k[i][2], o.a.k[i][3])};
    pk.v[i] = u32x2{pack2(o.a.v[i][0], o.a.v[i][1]), pack2(o.a.v[i][2], o.a.v[i][3])};
  }
}

__device__ __forceinline__ void attn_epilogue(int lane, int W, int mt, int hg, AttnPacked& pk, unsigned char* __restrict__ ws) {
  OPAQUE(lane);
  const int c = lane & 15, g = lane >> 4;
  f32x4 mprev, mcur, mfirst;
#pragma unroll
  for (int r = 0; r < 4; ++r) {
    int dcur = c - 4 * g - r, dprev = dcur + 16;
    mcur[r] = (dcur >= 0 && (dcur & 1) == 0) ? 0.f : -1e30f;
    mprev[r] = (dprev <= 14 && (dprev & 1) == 0) ? 0.f : -1e30f;
    mfirst[r] = mt == 0 ? -1e30f : mprev[r];
  }
  const float c2 = 0.22360679774997896f * 1.4426950408889634f;
  const unsigned sel_lo = (c & 1) ? 0x05040c0cu : 0x0c0c0504u, sel_hi = (c & 1) ? 0x07060c0cu : 0x0c0c0706u;
  unsigned exa = EX_OFF + W * 128 + c * 8 + g * 2, exv = EX_V + (g * 16 + W * 4 + (c & 3)) * 8;
  unsigned exg = K1_G + c * 4;
  OPAQUE(exa); OPAQUE(exv); OPAQUE(exg);
  WAIT_L0();
  if (W < 3 && lane == 0) __hip_atomic_fetch_add((LAS unsigned*)(uintptr_t)(K1_NEWS + 4), 1u, __ATOMIC_RELAXED, __HIP_MEMORY_SCOPE_WORKGROUP);
#pragma unroll
  for (int i = 0; i < 9; ++i) { if (i < 8) OPAQUE(pk.q[i]); OPAQUE(pk.k[i]); OPAQUE(pk.v[i]); }
  OPAQUE(mprev); OPAQUE(mcur); OPAQUE(mfirst);
  lds_wait(1, 3u);
  asm volatile("" ::: "memory");
  unsigned kx[9], qx[8]; u32x2 vx[9];
#pragma unroll
  for (int i = 0; i < 9; ++i) {
    kx[i] = *(const LAS unsigned short*)(uintptr_t)(exa + (EX_K - EX_OFF) + i * 512);
    if (i < 8) qx[i] = *(const LAS unsigned short*)(uintptr_t)(exa + (EX_Q - EX_OFF) + i * 512);
  }
#pragma unroll
  for (int i = 0; i < 9; ++i) vx[i] = lds_read64(exv + i * 512);
  float gt[8];
#pragma unroll
  for (int i = 0; i < 8; ++i) gt[i] = *(const LAS float*)(uintptr_t)(exg + i * 64);
  float t[8][4], m[8];
  f32x4 sp_[8], sc_[8];
#pragma unroll
  for (int i = 0; i < 8; ++i) {
    const int ip = i == 0 ? 8 : i - 1;
    const u32x4 qf = {pk.q[i].x, pk.q[i].y, qx[i], 0u};
    sp_[i] = mfma16(u32x4{pk.k[ip].x, pk.k[ip].y, kx[ip], 0u}, qf, i == 0 ? mfirst : mprev);
    sc_[i] = mfma16(u32x4{pk.k[i].x, pk.k[i].y, kx[i], 0u}, qf, mcur);
  }
  SCHED();
#pragma unroll
  for (int i = 0; i < 8; ++i) {
    const f32x4 sp = sp_[i], sc = sc_[i];
    t[i][0] = fmx(sp[0], sp[1]); t[i][1] = fmx(sp[2], sp[3]); t[i][2] = fmx(sc[0], sc[1]); t[i][3] = fmx(sc[2], sc[3]);
    m[i] = fmx(fmx(t[i][0], t[i][1]), fmx(t[i][2], t[i][3]));
  }
  bcast4(reduce4<true>(m[0], m[1], m[2], m[3]) * -c2, m[0], m[1], m[2], m[3]);
  bcast4(reduce4<true>(m[4], m[5], m[6], m[7]) * -c2, m[4], m[5], m[6], m[7]);
  float l[8];
#pragma unroll
  for (int i = 0; i < 8; ++i) {
#pragma unroll
    for (int r = 0; r < 4; ++r) t[i][r] = __builtin_amdgcn_exp2f(fmaf(t[i][r], c2, m[i]));
    l[i] = (t[i][0] + t[i][1]) + (t[i][2] + t[i][3]);
  }
  bcast4(__builtin_amdgcn_rcpf(reduce4<false>(l[0], l[1], l[2], l[3])), l[0], l[1], l[2], l[3]);
  bcast4(__builtin_amdgcn_rcpf(reduce4<false>(l[4], l[5], l[6], l[7])), l[4], l[5], l[6], l[7]);
  const int H = hg * 4 + W;
  const rsrc_t ro = out_rsrc(ws + A2_OFF, (unsigned)A2_BYTES);
  unsigned vm = (mt * 2 * KS2 + (H >> 1)) * 4096 + (H & 1) * 512 + (g & 1) * 1024 + (g >> 1) * 256 + c * 16;
  unsigned vx_ = (mt * 2 * KS2 + 16 + (hg >> 1)) * 4096 + (hg & 1) * 512 + g * 1024 + (W >> 1) * 256 + c * 16 + (W & 1) * 8;
  u32x4 pf[8];
#pragma unroll
  for (int i = 0; i < 8; ++i) {
    const float sc = l[i] * gt[i];
    const unsigned p01 = pack2(t[i][0] * sc, t[i][1] * sc), p23 = pack2(t[i][2] * sc, t[i][3] * sc);
    pf[i] = u32x4{__builtin_amdgcn_perm(p01, 0u, sel_lo), __builtin_amdgcn_perm(p01, 0u, sel_hi),
                  __builtin_amdgcn_perm(p23, 0u, sel_lo), __builtin_amdgcn_perm(p23, 0u, sel_hi)};
  }
  f32x4 om[8], ox[8];
#pragma unroll
  for (int i = 0; i < 8; ++i) {
    const int ip = i == 0 ? 8 : i - 1;
    const f32x4 z = {0.f, 0.f, 0.f, 0.f};
    om[i] = mfma16(cat(pk.v[ip], pk.v[i]), pf[i], z);
    ox[i] = mfma16(cat(vx[ip], vx[i]), pf[i], z);
  }
  SCHED();
  const bool g1 = g & 1, g2 = g & 2;
#pragma unroll
  for (int h = 0; h < 2; ++h) {
    u32x2 sm[4];
    unsigned ex[4], ey[4];
#pragma unroll
    for (int i = 0; i < 4; ++i) {
      sm[i] = u32x2{pack2(om[4 * h + i][0], om[4 * h + i][1]), pack2(om[4 * h + i][2], om[4 * h + i][3])};
      ex[i] = pack2(ox[4 * h + i][0], ox[4 * h + i][1]); ey[i] = pack2(ox[4 * h + i][2], ox[4 * h + i][3]);
    }
    bst16<ST_AUX>(ro, vm, widen_pair(sm[0], sm[1]));
    bst16<ST_AUX>(ro, vm + 2048, widen_pair(sm[2], sm[3]));
    unsigned x01 = g1 ? ex[1] : ex[0], x23 = g1 ? ex[3] : ex[2], y01 = g1 ? ey[1] : ey[0], y23 = g1 ? ey[3] : ey[2];
    OPAQUE(x01); OPAQUE(x23); OPAQUE(y01); OPAQUE(y23);
    bst8<ST_AUX>(ro, vx_, 0, u32x2{g2 ? x23 : x01, g2 ? y23 : y01});
    vm += KS2 * 4096; vx_ += KS2 * 4096;
  }
}

__device__ __forceinline__ unsigned mish2(float h0, float h1, float sc) {
  f32x2 h = {h0, h1};
  f32x2 a = h * 1.4426950408889634f;
  f32x2 w = {__builtin_amdgcn_exp2f(a.x), __builtin_amdgcn_exp2f(a.y)};
  w = w + 1.f;
  f32x2 d = __builtin_elementwise_fma(w, w, f32x2{1.f, 1.f});
  f32x2 r = {__builtin_amdgcn_rcpf(d.x), __builtin_amdgcn_rcpf(d.y)};
  f32x2 t = __builtin_elementwise_fma(r, f32x2{-2.f * sc, -2.f * sc}, f32x2{sc, sc});
  f32x2 y = h * t;
  return pack2(y.x, y.y);
}

typedef unsigned long long u64;
typedef __attribute__((address_space(1))) u64 gu64;
__device__ __forceinline__ u64 sy_ld(const gu64* p) { return __hip_atomic_load(p, __ATOMIC_RELAXED, __HIP_MEMORY_SCOPE_AGENT); }
__device__ __forceinline__ void sy_st(gu64* p, u64 v) { __hip_atomic_store(p, v, __ATOMIC_RELAXED, __HIP_MEMORY_SCOPE_AGENT); }

__device__ __forceinline__ void mlp_role(int lane, int wave, int mt, int nb0, const u32x2 (&bias)[7],
                                         unsigned char* __restrict__ ws, gu64* early, unsigned aep) {
  const int c = lane & 15, g = lane >> 4, q4 = (lane >> 2) & 3, p4 = lane & 3;
  const int wm = wave >> 1, wn = wave & 1;
  MlpOps o;
#pragma unroll
  for (int n = 0; n < 7; ++n) {
    f32x4 b = {bf_lo(bias[n].x), bf_hi(bias[n].x), bf_lo(bias[n].y), bf_hi(bias[n].y)};
#pragma unroll
    for (int r = 0; r < 4; ++r) o.a.c[r][n] = b;
  }
  o.xa = wm * 4 * 2048 + c * 128 + ((g ^ ((c >> 1) & 7)) * 16);
  o.wa = X_BYTES + ((g >> 1) * 16 + (g & 1) * 4 + q4) * W_ROW + wn * 8 * 32 + 8 * p4;
  o.d6 = wn ? (unsigned)-32 : 192u;
  STAMP(ws, 0, 1);
  k_loop(o);
  STAMP(ws, 0, 2);

  const rsrc_t ro = out_rsrc(ws + A2_OFF, (unsigned)A2_BYTES);
  const unsigned so = ((mt * 2 + wm) * KS2 + 20 + (nb0 >> 1)) * 4096;
  const unsigned vo = lane * 16;
  float sc[4];
#pragma unroll
  for (int r = 0; r < 4; ++r) sc[r] = 1.f - *(const LAS float*)(uintptr_t)(K1_G + (wm * 64 + r * 16 + c) * 4);
#pragma unroll
  for (int j = 0; j < 3; ++j) {
    unsigned vj = vo + so + (wn * 4 + j) * 4096;
    if (j == 2 && wn == 1 && nb0 >= 84) vj = 0x40000000u;
    OPAQUE(vj);
#pragma unroll
    for (int r = 0; r < 4; ++r) {
      const f32x4 h0 = o.a.c[r][2 * j], h1 = o.a.c[r][2 * j + 1];
      bst16<ST_AUX>(ro, vj + r * 1024,
                    u32x4{mish2(h0[0], h0[1], sc[r]), mish2(h0[2], h0[3], sc[r]), mish2(h1[0], h1[1], sc[r]), mish2(h1[2], h1[3], sc[r])});
    }
    SCHED();
  }
  WAIT_V(4);
  if (lane == 0) sy_st(early, *(const LAS u64*)(uintptr_t)aep + 1);
  const unsigned v6 = so + 3 * 4096 + wn * 512 + (g & 1) * 1024 + (g >> 1) * 256 + c * 16;
#pragma unroll
  for (int r = 0; r < 4; r += 2) {
    const f32x4 h0 = o.a.c[r][6], h1 = o.a.c[r + 1][6];
    bst16<ST_AUX>(ro, v6 + r * 1024, widen_pair(u32x2{mish2(h0[0], h0[1], sc[r]), mish2(h0[2], h0[3], sc[r])},
                                                u32x2{mish2(h1[0], h1[1], sc[r + 1]), mish2(h1[2], h1[3], sc[r + 1])}));
  }
}

__device__ __forceinline__ int w2cat_row(int beta, int rho) {
  const int r0 = 20 * beta + rho, r1 = 20 * (4 * (beta - 32) + (rho >> 2)) + 16 + (rho & 3), r2 = 16 * (beta - 40) + rho;
  return beta < 32 ? r0 : (beta < 40 ? r1 : r2);
}

__device__ __forceinline__ int kb_of(int t) { return 20 + 7 * t - (t == 7); }
__device__ __forceinline__ int ks_front(int w, int n) {
  if (n < 3) { const int e = 20 + 3 * w + n; return kb_of(e >> 2) + (e & 1) + 2 * (e & 2); }
  if (n < 7) return kb_of(2 * w + ((n - 3) >> 1)) + 2 + ((n - 3) & 1);
  return 4 + 4 * w + n - 7;
}
__device__ __forceinline__ int ks_back(int lw, int n) {
  if (n < 5) return kb_of(n) + (lw & 1) + 2 * (lw & 2);
  const int q = 4 * (n - 5) + lw;
  return q < 6 ? kb_of(q) + 6 : (q < 10 ? q - 6 : 0x40000);
}
constexpr int P2_REG = 7 * 5120;
static_assert(4 * P2_REG <= EX_OFF, "phase 2 re-uses the phase 1 ring");

typedef u32x4 __attribute__((aligned(8))) u32x4_a8;
constexpr int POLL_SLEEP = 4;
__device__ __forceinline__ void flags_wait(const gu64* f, const gu64* bs) {
  const u64 want = sy_ld(bs) + 1;
#pragma unroll 1
  for (int i = 0; i < SPIN_CAP; ++i) { if (__all(sy_ld(f) == want)) break; __builtin_amdgcn_s_sleep(POLL_SLEEP); }
}
__device__ __forceinline__ bool fb_up(const gu64* sy, int lane) {
  bool ok = true;
#pragma unroll
  for (int i = 0; i < 4; ++i) ok &= sy_ld(sy + SY_FB + 64 * i + lane) == sy_ld(sy + SY_BASE + 64 * i + lane) + 1;
  return __all(ok);
}
#define ACQUIRE() __builtin_amdgcn_fence(__ATOMIC_ACQUIRE, "wavefront")
__device__ __forceinline__ void ld16_dev(u32x4& r, unsigned voff, u32x4 rsrc, unsigned soff) {
  asm volatile("buffer_load_dwordx4 %0, %1, %2, %3 offen sc1" : "=v"(r) : "v"(voff), "s"(rsrc), "s"(soff) : "memory");
}
__device__ __forceinline__ void ld8_dev(u64& r, const gu64* p) { asm volatile("global_load_dwordx2 %0, %1, off sc1" : "=v"(r) : "v"(p) : "memory"); }
__device__ __forceinline__ void ld8_buf(u64& r, unsigned voff, u32x4 rsrc, unsigned soff) {
  asm volatile("buffer_load_dwordx2 %0, %1, %2, %3 offen sc1" : "=v"(r) : "v"(voff), "s"(rsrc), "s"(soff) : "memory");
}
#define PIN(x) asm volatile("" : "+v"(x))
template <int N> __device__ __forceinline__ void wait_v() { asm volatile("s_waitcnt vmcnt(%0)" :: "i"(N) : "memory"); }

__device__ __forceinline__ void mma20(f32x4 (&acc)[4][5], const u32x4 (&a)[4], const u32x4 (&b)[5]) {
#pragma unroll
  for (int i = 0; i < 4; ++i)
#pragma unroll
    for (int j = 0; j < 5; ++j) asm volatile("v_mfma_f32_16x16x32_bf16 %0, %1, %2, %0" : "+v"(acc[i][j]) : "v"(b[j]), "v"(a[i]));
}
__device__ __forceinline__ void acc_zero(f32x4 (&acc)[4][5]) {
#pragma unroll
  for (int i = 0; i < 4; ++i)
#pragma unroll
    for (int j = 0; j < 5; ++j) { acc[i][j] = f32x4{0.f, 0.f, 0.f, 0.f}; OPAQUE(acc[i][j]); }
  asm volatile("s_nop 1");
}
#define ACC_OUT() do { SCHED(); asm volatile("s_nop 7\n\ts_nop 3"); SCHED(); } while (0)
template <int N> __device__ __forceinline__ void wait_pin(u32x4 (&a)[4]) {
  asm volatile("s_waitcnt vmcnt(%4)" : "+v"(a[0]), "+v"(a[1]), "+v"(a[2]), "+v"(a[3]) : "i"(N) : "memory");
}

struct Stream2 {
  u32x4 rs;
  unsigned va[4], vo, sa, sb;
  int lw;
  u32x4 fb[5][5], fa[4][4];
  f32x4 acc[4][5];
  template <int N> __device__ __forceinline__ void load_b() {
    const unsigned so = sb + ks_back(lw, N) * 5120;
#pragma unroll
    for (int j = 0; j < 5; ++j) ld16_dev(fb[N % 5][j], vo, rs, so + j * 1024);
  }
  template <int N> __device__ __forceinline__ void load_a() {
    const unsigned so = sa + ks_back(lw, N) * 4096;
#pragma unroll
    for (int i = 0; i < 4; ++i) ld16_dev(fa[N % 4][i], va[i], rs, so);
  }
  template <int N, int YOUNGER> __device__ __forceinline__ void step() {
    wait_pin<YOUNGER>(fa[N % 4]);
#pragma unroll
    for (int j = 0; j < 5; ++j) PIN(fb[N % 5][j]);
    mma20(acc, fa[N % 4], fb[N % 5]);
    SCHED();
  }
  __device__ __forceinline__ void run() {
    load_a<0>(); load_a<1>(); load_a<2>(); load_a<3>();
    step<0, 12>(); step<1, 8>();
    load_a<4>(); load_b<5>(); lds_wait(0, 2u); ACQUIRE(); load_a<5>(); load_b<6>();
    step<2, 22>(); step<3, 18>();
    load_b<7>(); lds_wait(0, 3u); ACQUIRE(); load_a<6>(); load_a<7>();
    step<4, 27>(); step<5, 18>(); step<6, 4>(); step<7, 0>();
    ACC_OUT();
  }
};

__global__ __launch_bounds__(512, 2) void fused_kernel(const unsigned short* __restrict__ x, const unsigned short* __restrict__ wqkv,
                                                       const unsigned short* __restrict__ wo, const unsigned short* __restrict__ w1,
                                                       const unsigned short* __restrict__ b1, const unsigned short* __restrict__ w2,
                                                       const unsigned short* __restrict__ gate_w, unsigned char* ws,
                                                       unsigned short* __restrict__ out) {
  extern __shared__ __attribute__((aligned(16))) unsigned char smem[];
  const int lane = threadIdx.x & 63;
  const int wave = __builtin_amdgcn_readfirstlane(threadIdx.x >> 6);
  const int b = blockIdx.x, xcd = b & 7, idx = b >> 3;
  const int mt = (xcd & 1) * 8 + (idx & 7);
  const int nsel = idx >> 3;
  const int nt = (xcd >> 1) * 2 + (nsel & 1);
  const bool attn = nsel < 2;
  const int m0 = mt * 128;
  const int nb0 = nt < 7 ? nt * 14 : 96;
  const int L = mt * 16 + (attn ? 0 : 8) + nt;
  const int mt2 = (xcd & 3) * 8 + (idx & 7);
  const int nt2 = (xcd >> 2) * 4 + (idx >> 3);
  gu64* sy = (gu64*)(ws + SY_OFF);
  STAMP(ws, 0, 0);
  if (idx == 16 && wave < 4) {
    uint64_t pc;
    asm volatile("s_getpc_b64 %0" : "=s"(pc));
    const u32x4 rc = make_rsrc((const void*)(pc & ~(uint64_t)255), CODE_BYTES);
    dma_touch(K1_PF, (unsigned)(wave * 64 + lane) * 128u, rc, 0);
    dma_touch(K1_PF, (unsigned)(wave * 64 + lane) * 128u, rc, 32768);
  }

  if (__builtin_expect(wave >= 4, 1)) {
    const int lw = wave - 4;
    u64 pf[4], pb[4];
    {
      Stager st;
      st.rx = make_rsrc(x, S_TOK * D * 2);
      st.vx = (unsigned)((8 * lw + (lane >> 3)) * (D * 2) + (((lane & 7) ^ ((4 * lw + (lane >> 4)) & 7)) * 16));
      st.sx = (unsigned)(m0 * D * 2);
      st.lx = lw * 1024;
      stage_x(st, 0);
      if (lw == 0) ld8_dev(pb[0], sy + SY_BASE + L);
      st.rw = make_rsrc(attn ? wqkv : w1, attn ? D * 3 * D * 2 : D * F * 2);
      const unsigned rowbytes = attn ? 3 * D * 2 : F * 2, secstride = attn ? D * 2 - 160 : 0;
      const int chmax = attn ? 29 : (nt < 6 ? 27 : 23);
      st.wstep = 64u * rowbytes;
      st.sw = attn ? nt * 160 : nb0 * 32;
      st.lw = X_BYTES + lw * 1024;
#pragma unroll
      for (int j = 0; j < 7; ++j) st.vw[j] = w_src(lw + 4 * j, lane, rowbytes, secstride, chmax);
      {
        unsigned v4w = w_src(26 + lw, lane, rowbytes, secstride, chmax);
        const bool hx = lw < 2;
        st.v4 = hx ? st.vx : v4w;
        st.r4 = hx ? st.rx : st.rw;
        st.s4 = hx ? (attn ? (m0 ? st.sx - 16 * D * 2 : 0u) : 0x40000000u) : st.sw;
        st.step4 = hx ? 128u : st.wstep;
        st.l4 = hx ? (16 + lw) * 1024 : X_BYTES + (26 + lw) * 1024;
      }
      stage_w(st, 0); stage_next(st);
      stage_x(st, STAGE); stage_w(st, STAGE); stage_next(st);
      constexpr int T0 = 0;
      const int unit = lw < 2 ? lw * 256 + b : 384 + b;
      const bool tile = lw < 2 || (lw == 2 && b >= 128 && b < 208);
      const int uks = unit >> 3;
      const unsigned lt = EX_OFF + lw * 5120;
      unsigned vt[5];
#pragma unroll
      for (int i = 0; i < 5; ++i) {
        unsigned ci = i * 64 + lane, row = (ci * 205u) >> 11, ch = ci - row * 10u;
        vt[i] = (unsigned)w2cat_row(2 * uks + (row >> 4), row & 15) * (D * 2) + ch * 16;
        OPAQUE(vt[i]);
      }
      WAIT_V(12);
      STAMP(ws, 0, 1);
      BAR();
      stage_x(st, 2 * STAGE); stage_w(st, 2 * STAGE); stage_next(st);
      unsigned sb = 0;
#pragma unroll 1
      for (int t = 0; t < NT1 - 3; ++t) {
        if (tile && (t == T0 + 1 || t == T0 + 3)) WAIT_V(17); else WAIT_V(12);
        BAR();
        if (t == T0 + 4 && lw == 0) { PIN(pb[0]); if (lane == 0) sy_st(sy + SY_FB + L, pb[0] + 1); }
        stage_x(st, sb); stage_w(st, sb); stage_next(st);
        if (t == T0 && tile) {
          const u32x4 r2 = make_rsrc(uks < 20 ? wo : w2, uks < 20 ? D * D * 2 : F * D * 2);
#pragma unroll
          for (int i = 0; i < 5; ++i) dma16(lt + i * 1024, vt[i], r2, (unit & 7) * 160);
        }
        if (t == T0 + 2 && tile) {
          const bool half = uks < 20 || uks == 71 || (uks < 66 && (uks - 23) % 7 == 0);
          const unsigned ra = lt + ((half ? 8 : 4) * (lane >> 4) + ((lane >> 2) & 3)) * 160 + (lane & 3) * 40;
          const unsigned rb = ra + (half ? 4 : 16) * 160;
          unsigned char* bp = ws + B2_OFF + ((size_t)(unit & 7) * KS2 + uks) * 5120 + lane * 16;
#pragma unroll
          for (int i = 0; i < 5; ++i) st16(bp + i * 1024, cat(lds_tr(ra + 8 * i), lds_tr(rb + 8 * i)));
        }
        sb += STAGE; if (sb == NSTAGE * STAGE) sb = 0;
      }
    }
    Stream2 ld;
    ld.rs = make_rsrc(ws, (unsigned)(A2_BYTES + B2_BYTES + 8192));
    ld.vo = lane * 16;
#pragma unroll
    for (int i = 0; i < 4; ++i) ld.va[i] = lane * 16 + ((i + lw) & 3) * 1024;
    const unsigned sb0 = (unsigned)B2_OFF + nt2 * (KS2 * 5120);
    ld.sa = (unsigned)A2_OFF + mt2 * (KS2 * 4096); ld.sb = sb0; ld.lw = lw;
    WAIT_V(12); BAR();
    if (lw == 0) {
#pragma unroll
      for (int i = 0; i < 4; ++i) { ld8_dev(pf[i], sy + SY_FB + 64 * i + lane); ld8_dev(pb[i], sy + SY_BASE + 64 * i + lane); }
    }
    WAIT_V(0);
    if (lw == 0) {
      bool ok = true;
#pragma unroll
      for (int i = 0; i < 4; ++i) {
        PIN(pf[i]); PIN(pb[i]); ok &= pf[i] == pb[i] + 1;
        *(LAS u64*)(uintptr_t)(K1_BASES + i * 512 + lane * 8) = pb[i];
      }
      if (!__all(ok)) {
#pragma unroll 1
        for (int i = 0; i < SPIN_CAP; ++i) { if (fb_up(sy, lane)) break; __builtin_amdgcn_s_sleep(POLL_SLEEP); }
      }
      WAIT_L0();
    }
    BAR();
    ACQUIRE();
    if (lw == 1 || lw == 2)
      asm volatile("s_mov_b32 m0, %0\n\ts_nop 0\n\tbuffer_load_dword %1, %2, %3 offen sc1 lds"
                   :: "s"(K1_G + (lw - 1) * 256), "v"(lane * 4), "s"(ld.rs), "s"((unsigned)G_OFF + (m0 + (lw - 1) * 64) * 4) : "memory");
    ld.load_b<0>(); ld.load_b<1>(); ld.load_b<2>();
    WAIT_V(15); BAR();
    ld.load_b<3>(); ld.load_b<4>();
    STAMP(ws, 0, 2);
    auto stage_b = [&](int n) {
      const int ks = ks_front(lw, n);
#pragma unroll
      for (int j = 0; j < 5; ++j) dma16_dev(lw * P2_REG + n * 5120 + j * 1024, ld.vo, ld.rs, sb0 + ks * 5120 + j * 1024);
    };
    constexpr int THIN = 5;
    stage_b(0); stage_b(1);
    wait_v<THIN>(); stage_b(2); wait_v<THIN>(); stage_b(3); wait_v<THIN>(); stage_b(4); wait_v<THIN>();
    const int Lp = (mt2 >> 1) * 16 + 8 + ((lane >> 1) & 7);
    const gu64* fm = sy + SY_FE + Lp * 4 + (mt2 & 1) * 2 + (lane & 1);
    stage_b(5); wait_v<THIN>(); stage_b(6);
    if (lw == 0) { ld8_dev(pf[0], fm); ld8_dev(pb[0], sy + SY_BASE + Lp); }
    acc_zero(ld.acc);
    WAIT_V(0);
    lds_post(4 + lw, 1u, lane);
    STAMP(ws, 0, 3);
    if (lw == 0) {
      PIN(pf[0]); PIN(pb[0]);
#pragma unroll 1
      for (int k = 0; k < SPIN_CAP && !__all(pf[0] == pb[0] + 1); ++k) { __builtin_amdgcn_s_sleep(POLL_SLEEP); pf[0] = sy_ld(fm); }
      lds_post(0, 1u, lane);
    } else lds_wait(0, 1u);
    ACQUIRE();
    STAMP(ws, 0, 4);
    ld.run();
    STAMP(ws, 0, 5);
    lds_wait(8 + lw, 1u);
    unsigned rr = lw * P2_REG + lane * 16;
    OPAQUE(rr);
#pragma unroll
    for (int i = 0; i < 4; ++i)
#pragma unroll
      for (int j = 0; j < 5; ++j)
        if (i > 0 || j < 2) *(LAS f32x4*)(uintptr_t)(rr + (i == 0 ? 18 + j : 15 + i * 5 + j) * 1024) = ld.acc[i][j];
    WAIT_L0();
    BAR();
    f32x4 r[3];
#pragma unroll
    for (int j = 2; j < 5; ++j) r[j - 2] = ld.acc[0][j] + *(const LAS f32x4*)(uintptr_t)(rr + (j - 2) * 1024);
#pragma unroll
    for (int d = 1; d < 4; ++d) {
      unsigned rq = ((lw - d) & 3) * P2_REG + lane * 16;
      OPAQUE(rq);
#pragma unroll
      for (int j = 2; j < 5; ++j)
        r[j - 2] += *(const LAS f32x4*)(uintptr_t)(rq + (d * 5 + j - 2) * 1024) + *(const LAS f32x4*)(uintptr_t)(rq + (15 + d * 5 + j) * 1024);
    }
    unsigned* op = (unsigned*)(out + (size_t)(mt2 * 64 + lw * 16 + (lane & 15)) * D + nt2 * 80 + (lane >> 4) * 20);
    *(u32x4_a8*)(op + 4) = u32x4{pack2(r[0][0], r[0][1]), pack2(r[0][2], r[0][3]), pack2(r[1][0], r[1][1]), pack2(r[1][2], r[1][3])};
    *(u32x2*)(op + 8) = u32x2{pack2(r[2][0], r[2][1]), pack2(r[2][2], r[2][3])};
    STAMP(ws, 0, 6);
    return;
  }

  const unsigned aep = K1_BASES + L * 8;
  if (threadIdx.x < 16) *(LAS unsigned*)(uintptr_t)(K1_NEWS + threadIdx.x * 4) = 0u;
  u32x2 bias[7];
  if (!attn) {
    const u32x2* bp = (const u32x2*)(b1 + (lane >> 4) * 4);
#pragma unroll
    for (int n = 0; n < 7; ++n) bias[n] = bp[min(nb0 + (n < 6 ? (wave & 1) * 8 + n : 6 + (wave & 1)), 107) * 4];
  }
  if (attn) {
    const int tok = m0 + nt * 16 + wave * 4 + (lane >> 4);
    const u32x4* gp = (const u32x4*)gate_w + (lane & 15);
    const u32x4* xr = (const u32x4*)(x + (size_t)tok * D) + (lane & 15);
    float sg = 0.f;
#pragma unroll
    for (int j = 0; j < 5; ++j) {
      const u32x4 a = xr[16 * j], w = gp[16 * j];
#pragma unroll
      for (int e = 0; e < 4; ++e) { sg = fmaf(bf_lo(a[e]), bf_lo(w[e]), sg); sg = fmaf(bf_hi(a[e]), bf_hi(w[e]), sg); }
    }
#pragma unroll
    for (int o = 8; o >= 1; o >>= 1) sg += __shfl_xor(sg, o, 64);
    if ((lane & 15) == 0)
      __hip_atomic_store((__attribute__((address_space(1))) float*)(ws + G_OFF) + tok, 1.f / (1.f + __expf(-sg)),
                         __ATOMIC_RELAXED, __HIP_MEMORY_SCOPE_AGENT);
  }
  if (!attn) {
#pragma unroll
    for (int n = 0; n < 7; ++n) PIN(bias[n]);
  }
  if (!attn) {
    const int wg = b & 127;
    const int pw = wg * 4 + wave;
    const int li = pw * 28 + (lane < 28 ? lane : 0);
    dma_touch(K1_PF, (unsigned)((li / 7) * (D * 2) + (3 + li % 7) * 128), make_rsrc(x, S_TOK * D * 2), 0);
    {
      const int q = ((idx & 15) * 4 + wave) * 63 + (lane < 63 ? lane : 0), row = q / 9, sl = q - row * 9;
      dma_touch(K1_PF, (unsigned)(row * (3 * D * 2) + (sl / 3) * (D * 2) + (sl % 3) * 128 + (xcd >> 1) * 320), make_rsrc(wqkv, D * 3 * D * 2), 192 * 3 * D * 2);
    }
    dma_touch(K1_PF, (unsigned)((pw * 24 + (lane < 24 ? lane : 0)) * 128), make_rsrc(w1, D * F * 2), 192 * F * 2);
  }
  WAIT_L0();
  if (attn) {
    AttnPacked pk;
    if (wave == 0) attn_main<0>(lane, pk, ws);
    else if (wave == 1) attn_main<1>(lane, pk, ws);
    else if (wave == 2) attn_main<2>(lane, pk, ws);
    else attn_main<3>(lane, pk, ws);
    attn_epilogue(lane, wave, mt, nt, pk, ws);
  } else {
    mlp_role(lane, wave, mt, nb0, bias, ws, sy + SY_FE + L * 4 + wave, aep);
  }
  const unsigned ep_lo = __builtin_amdgcn_readfirstlane(*(const LAS unsigned*)(uintptr_t)aep);
  const unsigned ep_hi = __builtin_amdgcn_readfirstlane(*(const LAS unsigned*)(uintptr_t)(aep + 4));
  const u64 epoch = (((u64)ep_hi << 32) | ep_lo) + 1;
  WAIT_V(0);
  if (lane == 0) sy_st(sy + SY_FA + L * 4 + wave, epoch);
  STAMP(ws, 0, 3);

  int ln = lane;
  OPAQUE(ln);
  const u32x4 rs = make_rsrc(ws, (unsigned)SY_OFF + 32768);
  const unsigned sa = (unsigned)A2_OFF + mt2 * (KS2 * 4096);
  unsigned va[4];
#pragma unroll
  for (int i = 0; i < 4; ++i) va[i] = ln * 16 + ((i + wave) & 3) * 1024;
  u32x4 fa[7][4], ea[4][4], eb[4][5];
  const int La = (mt2 >> 1) * 16 + (ln < 32 ? (ln >> 2) : 8 + ((ln >> 1) & 7));
  const gu64* fat = sy + SY_FA + La * 4 + (ln < 32 ? (ln & 3) : (mt2 & 1) * 2 + (ln & 1));
  u64 pp[4];
  u64 up = 0;
  f32x4 acc[4][5];
  acc_zero(acc);
  unsigned rb = wave * P2_REG + ln * 16;
  OPAQUE(rb);
  const unsigned sbb = (unsigned)B2_OFF + nt2 * (KS2 * 5120), vo = ln * 16;
#define P2_STEP(n, CNT) { u32x4 fb[5]; _Pragma("unroll") for (int j = 0; j < 5; ++j) fb[j] = lds_read128(rb + n * 5120 + j * 1024); \
    wait_pin<CNT>(fa[n]); mma20(acc, fa[n], fb); SCHED(); }
#define P2_EXTRA_B(e) { _Pragma("unroll") for (int j = 0; j < 5; ++j) ld16_dev(eb[e][j], vo, rs, sbb + ks_front(wave, 7 + e) * 5120 + j * 1024); }
#define P2_EXTRA_A(e) { _Pragma("unroll") for (int i = 0; i < 4; ++i) ld16_dev(ea[e][i], va[i], rs, sa + ks_front(wave, 7 + e) * 4096); }
  P2_EXTRA_B(0) P2_EXTRA_B(1)
  lds_wait(4 + wave, 1u);
  lds_wait(0, 1u);
  ACQUIRE();
  if (wave == 0) {
    ld8_dev(pp[0], fat); ld8_dev(pp[1], sy + SY_BASE + La);
  }
#pragma unroll
  for (int n = 0; n < 3; ++n)
#pragma unroll
    for (int i = 0; i < 4; ++i) ld16_dev(fa[n][i], va[i], rs, sa + ks_front(wave, n) * 4096);
  if (wave == 0) {
    WAIT_V(12);
    PIN(pp[0]); PIN(pp[1]);
    up = __ballot(pp[0] == pp[1] + 1);
#pragma unroll 1
    for (int k = 0; k < SPIN_CAP && (up >> 32) != 0xffffffffull; ++k) { __builtin_amdgcn_s_sleep(POLL_SLEEP); up = __ballot(sy_ld(fat) == pp[1] + 1); }
    lds_post(0, up == ~0ull ? 3u : 2u, ln);
  }
  STAMP(ws, 0, 4);
  P2_STEP(0, 8)
  lds_wait(0, 2u);
  ACQUIRE();
  if (wave == 0) ld8_dev(pp[0], fat);
#pragma unroll
  for (int n = 3; n < 7; ++n)
#pragma unroll
    for (int i = 0; i < 4; ++i) ld16_dev(fa[n][i], va[i], rs, sa + ks_front(wave, n) * 4096);
  P2_STEP(1, 20)
  P2_STEP(2, 16)
  if (wave == 0) {
    PIN(pp[0]);
    up |= __ballot(pp[0] == pp[1] + 1);
#pragma unroll 1
    for (int k = 0; k < SPIN_CAP && up != ~0ull; ++k) { up = __ballot(sy_ld(fat) == pp[1] + 1); if (up != ~0ull) __builtin_amdgcn_s_sleep(POLL_SLEEP); }
    lds_post(0, 3u, ln);
    if (ln == 0) sy_st(sy + SY_PASS + L, epoch);
  }
  P2_EXTRA_B(2)
  P2_STEP(3, 17) P2_STEP(4, 13)
  lds_wait(0, 3u);
  ACQUIRE();
  P2_EXTRA_A(0) P2_EXTRA_A(1)
  P2_STEP(5, 17)
  P2_EXTRA_A(2)
  P2_STEP(6, 17)
  P2_EXTRA_B(3) P2_EXTRA_A(3)
#define P2_EXTRA(e, CNT) { wait_pin<CNT>(ea[e]); _Pragma("unroll") for (int j = 0; j < 5; ++j) PIN(eb[e][j]); mma20(acc, ea[e], eb[e]); SCHED(); }
  P2_EXTRA(0, 17) P2_EXTRA(1, 13) P2_EXTRA(2, 9) P2_EXTRA(3, 0)
#undef P2_EXTRA
  ACC_OUT();
#undef P2_STEP
#undef P2_EXTRA_A
#undef P2_EXTRA_B
  if (wave == 0) {
#pragma unroll
    for (int i = 0; i < 4; ++i) ld8_buf(pp[i], ln * 8, rs, (unsigned)SY_OFF + (SY_PASS + 64 * i) * 8);
  }
#pragma unroll
  for (int i = 0; i < 4; ++i)
#pragma unroll
    for (int j = 0; j < 5; ++j)
      if (i > 0 || j >= 2) *(LAS f32x4*)(uintptr_t)(rb + (i * 5 + j - 2) * 1024) = acc[i][j];
  WAIT_L0();
  lds_post(8 + wave, 1u, ln);
  STAMP(ws, 0, 5);
  BAR();
  {
    f32x4 r[2];
#pragma unroll
    for (int j = 0; j < 2; ++j) r[j] = acc[0][j] + *(const LAS f32x4*)(uintptr_t)(rb + (18 + j) * 1024);
#pragma unroll
    for (int d = 1; d < 4; ++d) {
      unsigned rq = ((wave - d) & 3) * P2_REG + ln * 16;
      OPAQUE(rq);
#pragma unroll
      for (int j = 0; j < 2; ++j)
        r[j] += *(const LAS f32x4*)(uintptr_t)(rq + (d * 5 + j - 2) * 1024) + *(const LAS f32x4*)(uintptr_t)(rq + (15 + d * 5 + j) * 1024);
    }
    if (wave == 0) WAIT_V(0);
    unsigned* op = (unsigned*)(out + (size_t)(mt2 * 64 + wave * 16 + (ln & 15)) * D + nt2 * 80 + (ln >> 4) * 20);
    *(u32x4_a8*)op = u32x4{pack2(r[0][0], r[0][1]), pack2(r[0][2], r[0][3]), pack2(r[1][0], r[1][1]), pack2(r[1][2], r[1][3])};
  }
  if (wave == 0) {
    u64 wb[4];
#pragma unroll
    for (int i = 0; i < 4; ++i) { PIN(pp[i]); wb[i] = *(const LAS u64*)(uintptr_t)(K1_BASES + i * 512 + ln * 8); }
#pragma unroll 1
    for (int k = 0; k < SPIN_CAP; ++k) {
      if (__all(pp[0] == wb[0] + 1 && pp[1] == wb[1] + 1 && pp[2] == wb[2] + 1 && pp[3] == wb[3] + 1)) break;
      __builtin_amdgcn_s_sleep(POLL_SLEEP);
#pragma unroll
      for (int i = 0; i < 4; ++i) pp[i] = sy_ld(sy + SY_PASS + 64 * i + ln);
    }
    if (ln == 0) sy_st(sy + SY_BASE + L, epoch);
  }
  STAMP(ws, 0, 6);
}

}

extern "C" void kernel_launch(void* const* d_in, const int* in_sizes, int n_in, void* d_out, int out_size, void* d_ws,
                              size_t ws_size, hipStream_t stream) {
  const unsigned short* x = (const unsigned short*)d_in[0];
  const unsigned short* wqkv = (const unsigned short*)d_in[1];
  const unsigned short* wo = (const unsigned short*)d_in[2];
  const unsigned short* w1 = (const unsigned short*)d_in[4];
  const unsigned short* b1 = (const unsigned short*)d_in[5];
  const unsigned short* w2 = (const unsigned short*)d_in[6];
  const unsigned short* gate_w = (const unsigned short*)d_in[7];
  (void)hipFuncSetAttribute((const void*)fused_kernel, hipFuncAttributeMaxDynamicSharedMemorySize, K1_LDS);
  fused_kernel<<<256, 512, K1_LDS, stream>>>(x, wqkv, wo, w1, b1, w2, gate_w, (unsigned char*)d_ws, (unsigned short*)d_out);
}
```

```cpp
#include <hip/hip_runtime.h>
#include <hip/hip_bf16.h>
#include <stdint.h>

#define STAMP(ws, kern, i)
#define CODE_BYTES 34816

namespace {

typedef __attribute__((ext_vector_type(4))) short s16x4;
typedef __attribute__((ext_vector_type(8))) __bf16 bf16x8;
typedef __attribute__((ext_vector_type(2))) __bf16 bf16x2;
typedef __attribute__((ext_vector_type(4))) float f32x4;
typedef __attribute__((ext_vector_type(2))) float f32x2;
typedef __attribute__((ext_vector_type(4))) unsigned u32x4;
typedef __attribute__((ext_vector_type(2))) unsigned u32x2;
#define LAS __attribute__((address_space(3)))

constexpr int S_TOK = 2048, D = 640, F = 1728;
constexpr int KS2 = 74;
constexpr size_t A2_BYTES = (size_t)128 * KS2 * 1024;
constexpr size_t B2_BYTES = (size_t)40 * KS2 * 1024;
constexpr size_t A2_OFF = 0, B2_OFF = A2_OFF + A2_BYTES, G_OFF = B2_OFF + B2_BYTES;
constexpr size_t SY_OFF = G_OFF + 8192;
constexpr int SY_BASE = 0, SY_PASS = 256, SY_FB = 512, SY_FA = 1024, SY_FE = 2048;

constexpr int X_BYTES = 144 * 128;
constexpr int W_ROW = 480;
constexpr int W_BYTES = 64 * W_ROW;
constexpr int STAGE = X_BYTES + W_BYTES;
constexpr int NSTAGE = 3;
constexpr int EX_OFF = NSTAGE * STAGE;
constexpr int EX_Q = EX_OFF, EX_K = EX_Q + 8 * 512, EX_V = EX_K + 9 * 512;
constexpr int K1_PF = EX_OFF + 3 * 5120;
constexpr int K1_BASES = EX_V + 9 * 512;
static_assert(K1_BASES + 2048 <= K1_PF, "LDS map");
constexpr int K1_NEWS = K1_PF + 256;
constexpr int K1_G = K1_NEWS + 64;
constexpr int K1_LDS = K1_G + 512;
constexpr int NT1 = 10;

__device__ __forceinline__ unsigned pack2(float a, float b) {
  f32x2 v = {a, b};
  bf16x2 r = __builtin_convertvector(v, bf16x2);
  return __builtin_bit_cast(unsigned, r);
}
__device__ __forceinline__ float bf_lo(unsigned u) { return __builtin_bit_cast(float, u << 16); }
__device__ __forceinline__ float bf_hi(unsigned u) { return __builtin_bit_cast(float, u & 0xffff0000u); }
__device__ __forceinline__ f32x4 mfma16(u32x4 a, u32x4 b, f32x4 c) {
  return __builtin_amdgcn_mfma_f32_16x16x32_bf16(__builtin_bit_cast(bf16x8, a), __builtin_bit_cast(bf16x8, b), c, 0, 0, 0);
}
__device__ __forceinline__ u32x4 lds_read128(unsigned addr) { return *(const LAS u32x4*)(uintptr_t)addr; }
__device__ __forceinline__ u32x2 lds_read64(unsigned addr) { return *(const LAS u32x2*)(uintptr_t)addr; }
__device__ __forceinline__ u32x2 lds_tr(unsigned addr) {
  s16x4 v = __builtin_amdgcn_ds_read_tr16_b64_v4i16((LAS s16x4*)(uintptr_t)addr);
  return __builtin_bit_cast(u32x2, v);
}
__device__ __forceinline__ void dma16(unsigned ldsaddr, unsigned voff, u32x4 rsrc, unsigned soff) {
  asm volatile("s_mov_b32 m0, %0\n\ts_nop 0\n\tbuffer_load_dwordx4 %1, %2, %3 offen lds"
               :: "s"(ldsaddr), "v"(voff), "s"(rsrc), "s"(soff) : "memory");
}
__device__ __forceinline__ void dma16_dev(unsigned ldsaddr, unsigned voff, u32x4 rsrc, unsigned soff) {
  asm volatile("s_mov_b32 m0, %0\n\ts_nop 0\n\tbuffer_load_dwordx4 %1, %2, %3 offen sc1 lds"
               :: "s"(ldsaddr), "v"(voff), "s"(rsrc), "s"(soff) : "memory");
}
__device__ __forceinline__ void dma_touch(unsigned ldsaddr, unsigned voff, u32x4 rsrc, unsigned soff) {
  asm volatile("s_mov_b32 m0, %0\n\ts_nop 0\n\tbuffer_load_dword %1, %2, %3 offen lds"
               :: "s"(ldsaddr), "v"(voff), "s"(rsrc), "s"(soff) : "memory");
}
__device__ __forceinline__ void st16(void* p, u32x4 v) {
  asm volatile("global_store_dwordx4 %0, %1, off sc1\n\ts_nop 1" :: "v"(p), "v"(v) : "memory");
}
constexpr int ST_AUX = 16;
typedef __amdgpu_buffer_rsrc_t rsrc_t;
__device__ __forceinline__ rsrc_t out_rsrc(void* p, unsigned bytes) { return __builtin_amdgcn_make_buffer_rsrc(p, 0, bytes, 0x00020000); }
template <int AUX> __device__ __forceinline__ void bst16(rsrc_t r, unsigned voff, u32x4 v) { __builtin_amdgcn_raw_buffer_store_b128(v, r, voff, 0, AUX); }
template <int AUX> __device__ __forceinline__ void bst8(rsrc_t r, unsigned voff, unsigned soff, u32x2 v) { __builtin_amdgcn_raw_buffer_store_b64(v, r, voff, soff, AUX); }
#define WAIT_V(n) asm volatile("s_waitcnt vmcnt(" #n ")" ::: "memory")
#define WAIT_L0() __builtin_amdgcn_s_waitcnt(0xC07F)
#define OPAQUE(x) asm volatile("" : "+v"(x))
#define BAR() do { asm volatile("" ::: "memory"); __builtin_amdgcn_s_barrier(); asm volatile("" ::: "memory"); } while (0)
#define SCHED() __builtin_amdgcn_sched_barrier(0)

__device__ __forceinline__ u32x4 make_rsrc(const void* p, unsigned bytes) {
  uint64_t a = (uint64_t)p;
  u32x4 r;
  r.x = __builtin_amdgcn_readfirstlane((unsigned)a);
  r.y = __builtin_amdgcn_readfirstlane((unsigned)(a >> 32) & 0xffffu);
  r.z = bytes;
  r.w = 0x00020000u;
  return r;
}

__device__ __forceinline__ int swap23(int k) { return (k & ~12) | ((k & 4) << 1) | ((k & 8) >> 1); }

struct Stager {
  unsigned vx, vw[7], v4;
  u32x4 rx, rw, r4;
  unsigned sx, sw, s4;
  unsigned wstep, step4;
  unsigned lx, lw, l4;
};
__device__ __forceinline__ void stage_x(const Stager& st, unsigned sb) {
#pragma unroll
  for (int p = 0; p < 4; ++p) dma16(sb + st.lx + p * 4096, st.vx, st.rx, st.sx + p * (32 * D * 2));
}
__device__ __forceinline__ void stage_w(const Stager& st, unsigned sb) {
#pragma unroll
  for (int j = 0; j < 7; ++j) dma16(sb + st.lw + j * 4096, st.vw[j], st.rw, st.sw);
  dma16(sb + st.l4, st.v4, st.r4, st.s4);
}
__device__ __forceinline__ void stage_next(Stager& st) { st.sx += 128u; st.sw += st.wstep; st.s4 += st.step4; }
__device__ __forceinline__ unsigned w_src(int wi, int lane, unsigned rowbytes, unsigned secstride, int chmax) {
  unsigned ci = wi * 64 + lane;
  unsigned pos = (ci * 2185u) >> 16, ch = ci - pos * 30u;
  unsigned sec = (ch * 205u) >> 11;
  unsigned k = swap23(pos);
  ch = ch < (unsigned)chmax ? ch : (unsigned)chmax;
  return k * rowbytes + ch * 16u + sec * secstride;
}

struct AttnAcc { f32x4 q[8], k[9], v[9], e[9]; };
__device__ __forceinline__ u32x4 cat(u32x2 a, u32x2 b) { return u32x4{a.x, a.y, b.x, b.y}; }

template <int W>
struct AttnOps {
  static constexpr int NE = W < 3 ? 1 : 3;
  struct Frags { u32x4 x[9]; u32x2 wl[3 + NE], wh[3 + NE]; };
  AttnAcc a;
  unsigned xa, wa, we;
  static constexpr int n_e(int rb) { return W == 0 ? rb < 7 : (W < 3 ? (rb == 8 || rb < 6) : (rb == 6 ? 2 : (rb == 7 ? 3 : 0))); }
  static constexpr int per_rb(int rb) { return 2 + (rb < 8 ? 1 : 0) + n_e(rb); }
  static constexpr int first(int o) { int c = 0; for (int i = 0; i < o; ++i) c += per_rb(i == 0 ? 8 : i - 1); return c; }
  static constexpr int pos_of(int I) { int o = 0; while (first(o + 1) <= I) ++o; return o; }
  static constexpr int NM = first(9);
  static constexpr int NR = 15 + 2 * NE;
  template <int I> __device__ __forceinline__ void mma(const Frags& f) {
    constexpr int o = pos_of(I), rb = o == 0 ? 8 : o - 1, j = I - first(o);
    if constexpr (j == 0) a.k[rb] = mfma16(cat(f.wl[1], f.wh[1]), f.x[rb], a.k[rb]);
    else if constexpr (j == 1) a.v[rb] = mfma16(f.x[rb], cat(f.wl[2], f.wh[2]), a.v[rb]);
    else if constexpr (j < 2 + n_e(rb)) {
      if constexpr (W == 2) a.e[rb] = mfma16(f.x[rb], cat(f.wl[3], f.wh[3]), a.e[rb]);
      else if constexpr (W < 2) a.e[rb] = mfma16(cat(f.wl[3], f.wh[3]), f.x[rb], a.e[rb]);
      else if constexpr (j == 2) a.e[rb - 5] = mfma16(cat(f.wl[4], f.wh[4]), f.x[rb], a.e[rb - 5]);
      else if constexpr (j == 3) a.e[rb - 3] = mfma16(f.x[rb], cat(f.wl[5], f.wh[5]), a.e[rb - 3]);
      else a.e[0] = mfma16(cat(f.wl[3], f.wh[3]), f.x[rb], a.e[0]);
    } else a.q[rb] = mfma16(cat(f.wl[0], f.wh[0]), f.x[rb], a.q[rb]);
  }
  __device__ __forceinline__ void init() {
#pragma unroll
    for (int i = 0; i < 9; ++i) {
      if (i < 8) a.q[i] = f32x4{0.f, 0.f, 0.f, 0.f};
      a.k[i] = f32x4{0.f, 0.f, 0.f, 0.f}; a.v[i] = f32x4{0.f, 0.f, 0.f, 0.f}; a.e[i] = f32x4{0.f, 0.f, 0.f, 0.f};
    }
  }
  unsigned x, w, e;
  __device__ __forceinline__ void bases(unsigned sb, int kk) {
    x = (xa ^ (kk * 64)) + sb; w = wa + sb + kk * 32 * W_ROW; e = we + sb + kk * 32 * W_ROW;
    OPAQUE(x); OPAQUE(w); OPAQUE(e);
  }
  template <int I> __device__ __forceinline__ void read(Frags& f) {
    if constexpr (I == 0) f.wl[1] = lds_tr(w + 160);
    if constexpr (I == 1) f.wh[1] = lds_tr(w + 160 + 8 * W_ROW);
    if constexpr (I == 2) f.x[8] = lds_read128(x + 8 * 2048);
    if constexpr (I == 3) f.wl[2] = lds_tr(w + 320);
    if constexpr (I == 4) f.wh[2] = lds_tr(w + 320 + 8 * W_ROW);
    if constexpr (I == 5) f.wl[3] = lds_tr(e);
    if constexpr (I == 6) f.wh[3] = lds_tr(e + 8 * W_ROW);
    if constexpr (I == 7) f.x[0] = lds_read128(x);
    if constexpr (I == 8) f.wl[0] = lds_tr(w);
    if constexpr (I == 9) f.wh[0] = lds_tr(w + 8 * W_ROW);
    if constexpr (I >= 10 && I < 17) f.x[I - 9] = lds_read128(x + (I - 9) * 2048);
    if constexpr (I >= 17) {
      constexpr int n = 4 + (I - 17) / 2;
      if constexpr ((I - 17) % 2 == 0) f.wl[n] = lds_tr(e + 160 * (n - 3)); else f.wh[n] = lds_tr(e + 160 * (n - 3) + 8 * W_ROW);
    }
  }
};

struct MlpAcc { f32x4 c[4][7]; };
struct MlpOps {
  struct Frags { u32x4 x[4]; u32x2 wl[7], wh[7]; };
  MlpAcc a;
  unsigned xa, wa;
  static constexpr int NM = 28, NR = 18;
  template <int I> __device__ __forceinline__ void mma(const Frags& f) {
    constexpr int n = I / 4, r = I % 4;
    a.c[r][n] = mfma16(cat(f.wl[n], f.wh[n]), f.x[r], a.c[r][n]);
  }
  u32x2 bias[7];
  __device__ __forceinline__ void init() {
#pragma unroll
    for (int n = 0; n < 7; ++n) {
      f32x4 b = {bf_lo(bias[n].x), bf_hi(bias[n].x), bf_lo(bias[n].y), bf_hi(bias[n].y)};
#pragma unroll
      for (int r = 0; r < 4; ++r) a.c[r][n] = b;
    }
  }
  unsigned x, w, w6, d6;
  __device__ __forceinline__ void bases(unsigned sb, int kk) {
    x = (xa ^ (kk * 64)) + sb; w = wa + sb + kk * 32 * W_ROW; w6 = w + d6;
    OPAQUE(x); OPAQUE(w); OPAQUE(w6);
  }
  template <int I> __device__ __forceinline__ void read(Frags& f) {
    if constexpr (I < 2) { if constexpr (I == 0) f.wl[0] = lds_tr(w); else f.wh[0] = lds_tr(w + 8 * W_ROW); }
    else if constexpr (I < 6) f.x[I - 2] = lds_read128(x + (I - 2) * 2048);
    else {
      constexpr int n = 1 + (I - 6) / 2;
      const unsigned a = n < 6 ? w + 32 * n : w6;
      if constexpr ((I & 1) == 0) f.wl[n] = lds_tr(a); else f.wh[n] = lds_tr(a + 8 * W_ROW);
    }
  }
};

template <class OPS, int I>
__device__ __forceinline__ void read_all(OPS& o, typename OPS::Frags& f) {
  if constexpr (I < OPS::NR) { o.template read<I>(f); read_all<OPS, I + 1>(o, f); }
}
template <class OPS, int I>
__device__ __forceinline__ void half_step(OPS& o, const typename OPS::Frags& fc, typename OPS::Frags& fn) {
  if constexpr (I < OPS::NM) {
    o.template mma<I>(fc);
    constexpr int span = OPS::NM - 6;
    static_assert(span >= OPS::NR, "one read per gap");
    constexpr int j = (I * OPS::NR + span - 1) / span;
    if constexpr (j < OPS::NR && j * span / OPS::NR == I) { SCHED(); o.template read<j>(fn); }
    SCHED();
    half_step<OPS, I + 1>(o, fc, fn);
  }
}

template <class OPS>
__device__ __forceinline__ void k_loop(OPS& o) {
  typename OPS::Frags f0, f1;
  o.init();
  o.bases(0, 0);
  BAR();
  read_all<OPS, 0>(o, f0);
  unsigned sb = 0;
#pragma unroll 1
  for (int t = 0; t < NT1; ++t) {
    unsigned sn = sb + STAGE; if (sn == NSTAGE * STAGE) sn = 0;
    o.bases(sb, 1);
    WAIT_L0();
    SCHED();
    half_step<OPS, 0>(o, f0, f1);
    o.bases(sn, 0);
    WAIT_L0();
    if (t == 2) WAIT_V(0);
    BAR();
    SCHED();
    half_step<OPS, 0>(o, f1, f0);
    sb = sn;
  }
}

__device__ __forceinline__ float fmx(float a, float b) { return __builtin_elementwise_maximum(a, b); }
__device__ __forceinline__ void swap32(float& a, float& b) {
  auto r = __builtin_amdgcn_permlane32_swap(__builtin_bit_cast(unsigned, a), __builtin_bit_cast(unsigned, b), false, false);
  unsigned r0 = r[0], r1 = r[1];
  a = __builtin_bit_cast(float, r0); b = __builtin_bit_cast(float, r1);
}
__device__ __forceinline__ void swap16(float& a, float& b) {
  auto r = __builtin_amdgcn_permlane16_swap(__builtin_bit_cast(unsigned, a), __builtin_bit_cast(unsigned, b), false, false);
  unsigned r0 = r[0], r1 = r[1];
  a = __builtin_bit_cast(float, r0); b = __builtin_bit_cast(float, r1);
}
__device__ __forceinline__ u32x4 widen_pair(u32x2 a, u32x2 b) {
  auto x = __builtin_amdgcn_permlane16_swap(a.x, b.x, false, false);
  auto y = __builtin_amdgcn_permlane16_swap(a.y, b.y, false, false);
  unsigned x0 = x[0], x1 = x[1], y0 = y[0], y1 = y[1];
  return u32x4{x0, y0, x1, y1};
}
template <bool MAX>
__device__ __forceinline__ float reduce4(float x0, float x1, float x2, float x3) {
  swap32(x0, x1); swap32(x2, x3);
  float y01 = MAX ? fmx(x0, x1) : x0 + x1, y23 = MAX ? fmx(x2, x3) : x2 + x3;
  swap16(y01, y23);
  return MAX ? fmx(y01, y23) : y01 + y23;
}
__device__ __forceinline__ void bcast4(float z, float& x0, float& x1, float& x2, float& x3) {
  float u = z, w = z;
  swap16(u, w);
  x0 = u; x1 = u; swap32(x0, x1);
  x2 = w; x3 = w; swap32(x2, x3);
}

constexpr int SPIN_CAP = 1 << 16;
__device__ __forceinline__ void lds_wait(int word, unsigned atleast) {
  volatile LAS unsigned* p = (volatile LAS unsigned*)(uintptr_t)(K1_NEWS + word * 4);
#pragma unroll 1
  for (int i = 0; i < 16 * SPIN_CAP; ++i) { if (__builtin_amdgcn_readfirstlane(*p) >= atleast) break; __builtin_amdgcn_s_sleep(1); }
}
__device__ __forceinline__ void lds_post(int word, unsigned v, int lane) {
  if (lane == 0) *(volatile LAS unsigned*)(uintptr_t)(K1_NEWS + word * 4) = v;
}

struct AttnPacked { u32x2 q[8], k[9], v[9]; };

template <int W>
__device__ __forceinline__ void attn_main(int lane, AttnPacked& pk, unsigned char* __restrict__ ws) {
  OPAQUE(lane);
  const int c = lane & 15, g = lane >> 4, q4 = (lane >> 2) & 3, p4 = lane & 3;
  AttnOps<W> o;
  o.xa = c * 128 + ((g ^ ((c >> 1) & 7)) * 16);
  const unsigned wrow = X_BYTES + ((g >> 1) * 16 + (g & 1) * 4 + q4) * W_ROW;
  o.wa = wrow + 40 * W + 8 * p4;
  o.we = wrow + 160 * (W < 3 ? W : 0) + 40 * p4 + 32;
  STAMP(ws, 0, 1);
  k_loop(o);
  STAMP(ws, 0, 2);
  OPAQUE(lane);
#pragma unroll
  for (int i = 0; i < 9; ++i) {
    constexpr int base = W == 0 ? EX_Q : (W == 1 ? EX_K : EX_V);
    constexpr int w3[5] = {EX_Q + 7 * 512, EX_K + 6 * 512, EX_K + 7 * 512, EX_V + 6 * 512, EX_V + 7 * 512};
    if (W < 3 ? AttnOps<W>::n_e(i) != 0 : i < 5) {
      u32x2 v = {pack2(o.a.e[i][0], o.a.e[i][1]), pack2(o.a.e[i][2], o.a.e[i][3])};
      *(LAS u32x2*)(uintptr_t)((W < 3 ? base + i * 512 : w3[i < 5 ? i : 0]) + lane * 8) = v;
    }
  }
#pragma unroll
  for (int i = 0; i < 9; ++i) {
    if (i < 8) pk.q[i] = u32x2{pack2(o.a.q[i][0], o.a.q[i][1]), pack2(o.a.q[i][2], o.a.q[i][3])};
    pk.k[i] = u32x2{pack2(o.a.k[i][0], o.a.k[i][1]), pack2(o.a.k[i][2], o.a.k[i][3])};
    pk.v[i] = u32x2{pack2(o.a.v[i][0], o.a.v[i][1]), pack2(o.a.v[i][2], o.a.v[i][3])};
  }
}

__device__ __forceinline__ void attn_epilogue(int lane, int W, int mt, int hg, AttnPacked& pk, unsigned char* __restrict__ ws) {
  OPAQUE(lane);
  const int c = lane & 15, g = lane >> 4;
  f32x4 mprev, mcur, mfirst;
#pragma unroll
  for (int r = 0; r < 4; ++r) {
    int dcur = c - 4 * g - r, dprev = dcur + 16;
    mcur[r] = (dcur >= 0 && (dcur & 1) == 0) ? 0.f : -1e30f;
    mprev[r] = (dprev <= 14 && (dprev & 1) == 0) ? 0.f : -1e30f;
    mfirst[r] = mt == 0 ? -1e30f : mprev[r];
  }
  const float c2 = 0.22360679774997896f * 1.4426950408889634f;
  const unsigned sel_lo = (c & 1) ? 0x05040c0cu : 0x0c0c0504u, sel_hi = (c & 1) ? 0x07060c0cu : 0x0c0c0706u;
  unsigned exa = EX_OFF + W * 128 + c * 8 + g * 2, exv = EX_V + (g * 16 + W * 4 + (c & 3)) * 8;
  unsigned exg = K1_G + c * 4;
  OPAQUE(exa); OPAQUE(exv); OPAQUE(exg);
  WAIT_L0();
  if (lane == 0) __hip_atomic_fetch_add((LAS unsigned*)(uintptr_t)(K1_NEWS + 4), 1u, __ATOMIC_RELAXED, __HIP_MEMORY_SCOPE_WORKGROUP);
#pragma unroll
  for (int i = 0; i < 9; ++i) { if (i < 8) OPAQUE(pk.q[i]); OPAQUE(pk.k[i]); OPAQUE(pk.v[i]); }
  OPAQUE(mprev); OPAQUE(mcur); OPAQUE(mfirst);
  lds_wait(1, 4u);
  asm volatile("" ::: "memory");
  unsigned kx[9], qx[8]; u32x2 vx[9];
#pragma unroll
  for (int i = 0; i < 9; ++i) {
    kx[i] = *(const LAS unsigned short*)(uintptr_t)(exa + (EX_K - EX_OFF) + i * 512);
    if (i < 8) qx[i] = *(const LAS unsigned short*)(uintptr_t)(exa + (EX_Q - EX_OFF) + i * 512);
  }
#pragma unroll
  for (int i = 0; i < 9; ++i) vx[i] = lds_read64(exv + i * 512);
  float gt[8];
#pragma unroll
  for (int i = 0; i < 8; ++i) gt[i] = *(const LAS float*)(uintptr_t)(exg + i * 64);
  float t[8][4], m[8];
  f32x4 sp_[8], sc_[8];
#pragma unroll
  for (int i = 0; i < 8; ++i) {
    const int ip = i == 0 ? 8 : i - 1;
    const u32x4 qf = {pk.q[i].x, pk.q[i].y, qx[i], 0u};
    sp_[i] = mfma16(u32x4{pk.k[ip].x, pk.k[ip].y, kx[ip], 0u}, qf, i == 0 ? mfirst : mprev);
    sc_[i] = mfma16(u32x4{pk.k[i].x, pk.k[i].y, kx[i], 0u}, qf, mcur);
  }
  SCHED();
#pragma unroll
  for (int i = 0; i < 8; ++i) {
    const f32x4 sp = sp_[i], sc = sc_[i];
    t[i][0] = fmx(sp[0], sp[1]); t[i][1] = fmx(sp[2], sp[3]); t[i][2] = fmx(sc[0], sc[1]); t[i][3] = fmx(sc[2], sc[3]);
    m[i] = fmx(fmx(t[i][0], t[i][1]), fmx(t[i][2], t[i][3]));
  }
  bcast4(reduce4<true>(m[0], m[1], m[2], m[3]) * -c2, m[0], m[1], m[2], m[3]);
  bcast4(reduce4<true>(m[4], m[5], m[6], m[7]) * -c2, m[4], m[5], m[6], m[7]);
  float l[8];
#pragma unroll
  for (int i = 0; i < 8; ++i) {
#pragma unroll
    for (int r = 0; r < 4; ++r) t[i][r] = __builtin_amdgcn_exp2f(fmaf(t[i][r], c2, m[i]));
    l[i] = (t[i][0] + t[i][1]) + (t[i][2] + t[i][3]);
  }
  bcast4(__builtin_amdgcn_rcpf(reduce4<false>(l[0], l[1], l[2], l[3])), l[0], l[1], l[2], l[3]);
  bcast4(__builtin_amdgcn_rcpf(reduce4<false>(l[4], l[5], l[6], l[7])), l[4], l[5], l[6], l[7]);
  const int H = hg * 4 + W;
  const rsrc_t ro = out_rsrc(ws + A2_OFF, (unsigned)A2_BYTES);
  unsigned vm = (mt * 2 * KS2 + (H >> 1)) * 4096 + (H & 1) * 512 + (g & 1) * 1024 + (g >> 1) * 256 + c * 16;
  unsigned vx_ = (mt * 2 * KS2 + 16 + (hg >> 1)) * 4096 + (hg & 1) * 512 + g * 1024 + (W >> 1) * 256 + c * 16 + (W & 1) * 8;
  u32x4 pf[8];
#pragma unroll
  for (int i = 0; i < 8; ++i) {
    const float sc = l[i] * gt[i];
    const unsigned p01 = pack2(t[i][0] * sc, t[i][1] * sc), p23 = pack2(t[i][2] * sc, t[i][3] * sc);
    pf[i] = u32x4{__builtin_amdgcn_perm(p01, 0u, sel_lo), __builtin_amdgcn_perm(p01, 0u, sel_hi),
                  __builtin_amdgcn_perm(p23, 0u, sel_lo), __builtin_amdgcn_perm(p23, 0u, sel_hi)};
  }
  f32x4 om[8], ox[8];
#pragma unroll
  for (int i = 0; i < 8; ++i) {
    const int ip = i == 0 ? 8 : i - 1;
    const f32x4 z = {0.f, 0.f, 0.f, 0.f};
    om[i] = mfma16(cat(pk.v[ip], pk.v[i]), pf[i], z);
    ox[i] = mfma16(cat(vx[ip], vx[i]), pf[i], z);
  }
  SCHED();
  const bool g1 = g & 1, g2 = g & 2;
#pragma unroll
  for (int h = 0; h < 2; ++h) {
    u32x2 sm[4];
    unsigned ex[4], ey[4];
#pragma unroll
    for (int i = 0; i < 4; ++i) {
      sm[i] = u32x2{pack2(om[4 * h + i][0], om[4 * h + i][1]), pack2(om[4 * h + i][2], om[4 * h + i][3])};
      ex[i] = pack2(ox[4 * h + i][0], ox[4 * h + i][1]); ey[i] = pack2(ox[4 * h + i][2], ox[4 * h + i][3]);
    }
    bst16<ST_AUX>(ro, vm, widen_pair(sm[0], sm[1]));
    bst16<ST_AUX>(ro, vm + 2048, widen_pair(sm[2], sm[3]));
    unsigned x01 = g1 ? ex[1] : ex[0], x23 = g1 ? ex[3] : ex[2], y01 = g1 ? ey[1] : ey[0], y23 = g1 ? ey[3] : ey[2];
    OPAQUE(x01); OPAQUE(x23); OPAQUE(y01); OPAQUE(y23);
    bst8<ST_AUX>(ro, vx_, 0, u32x2{g2 ? x23 : x01, g2 ? y23 : y01});
    vm += KS2 * 4096; vx_ += KS2 * 4096;
  }
}

__device__ __forceinline__ u32x4 mish8(f32x4 h0, f32x4 h1, float s0, float s1) {
  const f32x2 h[4] = {{h0[0], h0[1]}, {h0[2], h0[3]}, {h1[0], h1[1]}, {h1[2], h1[3]}};
  f32x2 w[4];
#pragma unroll
  for (int i = 0; i < 4; ++i) w[i] = h[i] * 1.4426950408889634f;
  SCHED();
#pragma unroll
  for (int i = 0; i < 4; ++i) w[i] = f32x2{__builtin_amdgcn_exp2f(w[i].x), __builtin_amdgcn_exp2f(w[i].y)};
  SCHED();
#pragma unroll
  for (int i = 0; i < 4; ++i) w[i] = w[i] + 1.f;
  SCHED();
#pragma unroll
  for (int i = 0; i < 4; ++i) w[i] = __builtin_elementwise_fma(w[i], w[i], f32x2{1.f, 1.f});
  SCHED();
#pragma unroll
  for (int i = 0; i < 4; ++i) w[i] = f32x2{__builtin_amdgcn_rcpf(w[i].x), __builtin_amdgcn_rcpf(w[i].y)};
  SCHED();
#pragma unroll
  for (int i = 0; i < 4; ++i) { const float sc = i < 2 ? s0 : s1; w[i] = __builtin_elementwise_fma(w[i], f32x2{-2.f * sc, -2.f * sc}, f32x2{sc, sc}); }
  SCHED();
#pragma unroll
  for (int i = 0; i < 4; ++i) w[i] = h[i] * w[i];
  SCHED();
  return u32x4{pack2(w[0].x, w[0].y), pack2(w[1].x, w[1].y), pack2(w[2].x, w[2].y), pack2(w[3].x, w[3].y)};
}

typedef unsigned long long u64;
typedef __attribute__((address_space(1))) u64 gu64;
__device__ __forceinline__ u64 sy_ld(const gu64* p) { return __hip_atomic_load(p, __ATOMIC_RELAXED, __HIP_MEMORY_SCOPE_AGENT); }
__device__ __forceinline__ void sy_st(gu64* p, u64 v) { __hip_atomic_store(p, v, __ATOMIC_RELAXED, __HIP_MEMORY_SCOPE_AGENT); }

__device__ __forceinline__ void mlp_role(int lane, int wave, int mt, int nb0, const u32x2 (&bias)[7],
                                         unsigned char* __restrict__ ws, gu64* early, unsigned aep) {
  const int c = lane & 15, g = lane >> 4, q4 = (lane >> 2) & 3, p4 = lane & 3;
  const int wm = wave >> 1, wn = wave & 1;
  MlpOps o;
#pragma unroll
  for (int n = 0; n < 7; ++n) o.bias[n] = bias[n];
  o.xa = wm * 4 * 2048 + c * 128 + ((g ^ ((c >> 1) & 7)) * 16);
  o.wa = X_BYTES + ((g >> 1) * 16 + (g & 1) * 4 + q4) * W_ROW + wn * 8 * 32 + 8 * p4;
  o.d6 = wn ? (unsigned)-32 : 192u;
  STAMP(ws, 0, 1);
  k_loop(o);
  STAMP(ws, 0, 2);

  const rsrc_t ro = out_rsrc(ws + A2_OFF, (unsigned)A2_BYTES);
  const unsigned so = ((mt * 2 + wm) * KS2 + 20 + (nb0 >> 1)) * 4096;
  const unsigned vo = lane * 16;
  float sc[4];
#pragma unroll
  for (int r = 0; r < 4; ++r) sc[r] = 1.f - *(const LAS float*)(uintptr_t)(K1_G + (wm * 64 + r * 16 + c) * 4);
#pragma unroll
  for (int j = 0; j < 3; ++j) {
    unsigned vj = vo + so + (wn * 4 + j) * 4096;
    if (j == 2 && wn == 1 && nb0 >= 84) vj = 0x40000000u;
    OPAQUE(vj);
#pragma unroll
    for (int r = 0; r < 4; ++r) {
      const f32x4 h0 = o.a.c[r][2 * j], h1 = o.a.c[r][2 * j + 1];
      bst16<ST_AUX>(ro, vj + r * 1024, mish8(h0, h1, sc[r], sc[r]));
    }
    SCHED();
  }
  WAIT_V(4);
  if (lane == 0) sy_st(early, *(const LAS u64*)(uintptr_t)aep + 1);
  const unsigned v6 = so + 3 * 4096 + wn * 512 + (g & 1) * 1024 + (g >> 1) * 256 + c * 16;
#pragma unroll
  for (int r = 0; r < 4; r += 2) {
    const f32x4 h0 = o.a.c[r][6], h1 = o.a.c[r + 1][6];
    const u32x4 m = mish8(h0, h1, sc[r], sc[r + 1]);
    bst16<ST_AUX>(ro, v6 + r * 1024, widen_pair(u32x2{m.x, m.y}, u32x2{m.z, m.w}));
  }
}

__device__ __forceinline__ int w2cat_row(int beta, int rho) {
  const int r0 = 20 * beta + rho, r1 = 20 * (4 * (beta - 32) + (rho >> 2)) + 16 + (rho & 3), r2 = 16 * (beta - 40) + rho;
  return beta < 32 ? r0 : (beta < 40 ? r1 : r2);
}

__device__ __forceinline__ int kb_of(int t) { return 20 + 7 * t - (t == 7); }
__device__ __forceinline__ int ks_front(int w, int n) {
  if (n < 3) { const int e = 20 + 3 * w + n; return kb_of(e >> 2) + (e & 1) + 2 * (e & 2); }
  if (n < 7) return kb_of(2 * w + ((n - 3) >> 1)) + 2 + ((n - 3) & 1);
  return 4 + 4 * w + n - 7;
}
__device__ __forceinline__ int ks_back(int lw, int n) {
  if (n < 5) return kb_of(n) + (lw & 1) + 2 * (lw & 2);
  const int q = 4 * (n - 5) + lw;
  return q < 6 ? kb_of(q) + 6 : (q < 10 ? q - 6 : 0x40000);
}
constexpr int P2_REG = 7 * 5120;
static_assert(4 * P2_REG <= EX_OFF, "phase 2 re-uses the phase 1 ring");

typedef u32x4 __attribute__((aligned(8))) u32x4_a8;
constexpr int POLL_SLEEP = 4;
__device__ __forceinline__ void flags_wait(const gu64* f, const gu64* bs) {
  const u64 want = sy_ld(bs) + 1;
#pragma unroll 1
  for (int i = 0; i < SPIN_CAP; ++i) { if (__all(sy_ld(f) == want)) break; __builtin_amdgcn_s_sleep(POLL_SLEEP); }
}
__device__ __forceinline__ bool fb_up(const gu64* sy, int lane) {
  bool ok = true;
#pragma unroll
  for (int i = 0; i < 4; ++i) ok &= sy_ld(sy + SY_FB + 64 * i + lane) == sy_ld(sy + SY_BASE + 64 * i + lane) + 1;
  return __all(ok);
}
#define ACQUIRE() __builtin_amdgcn_fence(__ATOMIC_ACQUIRE, "wavefront")
__device__ __forceinline__ void ld16_dev(u32x4& r, unsigned voff, u32x4 rsrc, unsigned soff) {
  asm volatile("buffer_load_dwordx4 %0, %1, %2, %3 offen sc1" : "=v"(r) : "v"(voff), "s"(rsrc), "s"(soff) : "memory");
}
__device__ __forceinline__ void ld8_dev(u64& r, const gu64* p) { asm volatile("global_load_dwordx2 %0, %1, off sc1" : "=v"(r) : "v"(p) : "memory"); }
__device__ __forceinline__ void ld8_buf(u64& r, unsigned voff, u32x4 rsrc, unsigned soff) {
  asm volatile("buffer_load_dwordx2 %0, %1, %2, %3 offen sc1" : "=v"(r) : "v"(voff), "s"(rsrc), "s"(soff) : "memory");
}
#define PIN(x) asm volatile("" : "+v"(x))
template <int N> __device__ __forceinline__ void wait_v() { asm volatile("s_waitcnt vmcnt(%0)" :: "i"(N) : "memory"); }

__device__ __forceinline__ void mma20(f32x4 (&acc)[4][5], const u32x4 (&a)[4], const u32x4 (&b)[5]) {
#pragma unroll
  for (int i = 0; i < 4; ++i)
#pragma unroll
    for (int j = 0; j < 5; ++j) asm volatile("v_mfma_f32_16x16x32_bf16 %0, %1, %2, %0" : "+v"(acc[i][j]) : "v"(b[j]), "v"(a[i]));
}
__device__ __forceinline__ void acc_zero(f32x4 (&acc)[4][5]) {
#pragma unroll
  for (int i = 0; i < 4; ++i)
#pragma unroll
    for (int j = 0; j < 5; ++j) { acc[i][j] = f32x4{0.f, 0.f, 0.f, 0.f}; OPAQUE(acc[i][j]); }
  asm volatile("s_nop 1");
}
#define ACC_OUT() do { SCHED(); asm volatile("s_nop 7\n\ts_nop 3"); SCHED(); } while (0)
template <int N> __device__ __forceinline__ void wait_pin(u32x4 (&a)[4]) {
  asm volatile("s_waitcnt vmcnt(%4)" : "+v"(a[0]), "+v"(a[1]), "+v"(a[2]), "+v"(a[3]) : "i"(N) : "memory");
}

struct Stream2 {
  u32x4 rs;
  unsigned va[4], vo, sa, sb;
  int lw;
  u32x4 fb[5][5], fa[4][4];
  f32x4 acc[4][5];
  template <int N> __device__ __forceinline__ void load_b() {
    const unsigned so = sb + ks_back(lw, N) * 5120;
#pragma unroll
    for (int j = 0; j < 5; ++j) ld16_dev(fb[N % 5][j], vo, rs, so + j * 1024);
  }
  template <int N> __device__ __forceinline__ void load_a() {
    const unsigned so = sa + ks_back(lw, N) * 4096;
#pragma unroll
    for (int i = 0; i < 4; ++i) ld16_dev(fa[N % 4][i], va[i], rs, so);
  }
  template <int N, int YOUNGER> __device__ __forceinline__ void step() {
    wait_pin<YOUNGER>(fa[N % 4]);
#pragma unroll
    for (int j = 0; j < 5; ++j) PIN(fb[N % 5][j]);
    mma20(acc, fa[N % 4], fb[N % 5]);
    SCHED();
  }
  __device__ __forceinline__ void run() {
    load_a<0>(); load_a<1>(); load_a<2>(); load_a<3>();
    step<0, 12>(); step<1, 8>();
    load_a<4>(); load_b<5>(); lds_wait(0, 2u); ACQUIRE(); load_a<5>(); load_b<6>();
    step<2, 22>(); step<3, 18>();
    load_b<7>(); lds_wait(0, 3u); ACQUIRE(); load_a<6>(); load_a<7>();
    step<4, 27>(); step<5, 18>(); step<6, 4>(); step<7, 0>();
    ACC_OUT();
  }
};

__global__ __launch_bounds__(512, 2) void fused_kernel(const unsigned short* __restrict__ x, const unsigned short* __restrict__ wqkv,
                                                       const unsigned short* __restrict__ wo, const unsigned short* __restrict__ w1,
                                                       const unsigned short* __restrict__ b1, const unsigned short* __restrict__ w2,
                                                       const unsigned short* __restrict__ gate_w, unsigned char* ws,
                                                       unsigned short* __restrict__ out) {
  extern __shared__ __attribute__((aligned(16))) unsigned char smem[];
  const int lane = threadIdx.x & 63;
  const int wave = __builtin_amdgcn_readfirstlane(threadIdx.x >> 6);
  const int b = blockIdx.x, xcd = b & 7, idx = b >> 3;
  const int mt = (xcd & 1) * 8 + (idx & 7);
  const int nsel = idx >> 3;
  const int nt = (xcd >> 1) * 2 + (nsel & 1);
  const bool attn = nsel < 2;
  const int m0 = mt * 128;
  const int nb0 = nt < 7 ? nt * 14 : 96;
  const int L = mt * 16 + (attn ? 0 : 8) + nt;
  const int mt2 = (xcd & 3) * 8 + (idx & 7);
  const int nt2 = (xcd >> 2) * 4 + (idx >> 3);
  gu64* sy = (gu64*)(ws + SY_OFF);
  STAMP(ws, 0, 0);
  if (idx == 16 && wave < 4) {
    uint64_t pc;
    asm volatile("s_getpc_b64 %0" : "=s"(pc));
    const u32x4 rc = make_rsrc((const void*)(pc & ~(uint64_t)255), CODE_BYTES);
    dma_touch(K1_PF, (unsigned)(wave * 64 + lane) * 128u, rc, 0);
    dma_touch(K1_PF, (unsigned)(wave * 64 + lane) * 128u, rc, 32768);
  }

  if (__builtin_expect(wave >= 4, 1)) {
    const int lw = wave - 4;
    u64 pf[4], pb[4];
    {
      Stager st;
      st.rx = make_rsrc(x, S_TOK * D * 2);
      st.vx = (unsigned)((8 * lw + (lane >> 3)) * (D * 2) + (((lane & 7) ^ ((4 * lw + (lane >> 4)) & 7)) * 16));
      st.sx = (unsigned)(m0 * D * 2);
      st.lx = lw * 1024;
      stage_x(st, 0);
      if (lw == 0) ld8_dev(pb[0], sy + SY_BASE + L);
      st.rw = make_rsrc(attn ? wqkv : w1, attn ? D * 3 * D * 2 : D * F * 2);
      const unsigned rowbytes = attn ? 3 * D * 2 : F * 2, secstride = attn ? D * 2 - 160 : 0;
      const int chmax = attn ? 29 : (nt < 6 ? 27 : 23);
      st.wstep = 64u * rowbytes;
      st.sw = attn ? nt * 160 : nb0 * 32;
      st.lw = X_BYTES + lw * 1024;
#pragma unroll
      for (int j = 0; j < 7; ++j) st.vw[j] = w_src(lw + 4 * j, lane, rowbytes, secstride, chmax);
      {
        unsigned v4w = w_src(26 + lw, lane, rowbytes, secstride, chmax);
        const bool hx = lw < 2;
        st.v4 = hx ? st.vx : v4w;
        st.r4 = hx ? st.rx : st.rw;
        st.s4 = hx ? (attn ? (m0 ? st.sx - 16 * D * 2 : 0u) : 0x40000000u) : st.sw;
        st.step4 = hx ? 128u : st.wstep;
        st.l4 = hx ? (16 + lw) * 1024 : X_BYTES + (26 + lw) * 1024;
      }
      stage_w(st, 0); stage_next(st);
      stage_x(st, STAGE); stage_w(st, STAGE); stage_next(st);
      constexpr int T0 = 0;
      const int unit = lw < 2 ? lw * 256 + b : 384 + b;
      const bool tile = lw < 2 || (lw == 2 && b >= 128 && b < 208);
      const int uks = unit >> 3;
      const unsigned lt = EX_OFF + lw * 5120;
      unsigned vt[5];
#pragma unroll
      for (int i = 0; i < 5; ++i) {
        unsigned ci = i * 64 + lane, row = (ci * 205u) >> 11, ch = ci - row * 10u;
        vt[i] = (unsigned)w2cat_row(2 * uks + (row >> 4), row & 15) * (D * 2) + ch * 16;
        OPAQUE(vt[i]);
      }
      WAIT_V(12);
      STAMP(ws, 0, 1);
      BAR();
      stage_x(st, 2 * STAGE); stage_w(st, 2 * STAGE); stage_next(st);
      unsigned sb = 0;
#pragma unroll 1
      for (int t = 0; t < NT1 - 3; ++t) {
        if (tile && (t == T0 + 1 || t == T0 + 3)) WAIT_V(17); else WAIT_V(12);
        BAR();
        if (t == T0 + 4 && lw == 0) { PIN(pb[0]); if (lane == 0) sy_st(sy + SY_FB + L, pb[0] + 1); }
        stage_x(st, sb); stage_w(st, sb); stage_next(st);
        if (t == T0 && tile) {
          const u32x4 r2 = make_rsrc(uks < 20 ? wo : w2, uks < 20 ? D * D * 2 : F * D * 2);
#pragma unroll
          for (int i = 0; i < 5; ++i) dma16(lt + i * 1024, vt[i], r2, (unit & 7) * 160);
        }
        if (t == T0 + 2 && tile) {
          const bool half = uks < 20 || uks == 71 || (uks < 66 && (uks - 23) % 7 == 0);
          const unsigned ra = lt + ((half ? 8 : 4) * (lane >> 4) + ((lane >> 2) & 3)) * 160 + (lane & 3) * 40;
          const unsigned rb = ra + (half ? 4 : 16) * 160;
          unsigned char* bp = ws + B2_OFF + ((size_t)(unit & 7) * KS2 + uks) * 5120 + lane * 16;
#pragma unroll
          for (int i = 0; i < 5; ++i) st16(bp + i * 1024, cat(lds_tr(ra + 8 * i), lds_tr(rb + 8 * i)));
        }
        sb += STAGE; if (sb == NSTAGE * STAGE) sb = 0;
      }
    }
    Stream2 ld;
    ld.rs = make_rsrc(ws, (unsigned)(A2_BYTES + B2_BYTES + 8192));
    ld.vo = lane * 16;
#pragma unroll
    for (int i = 0; i < 4; ++i) ld.va[i] = lane * 16 + ((i + lw) & 3) * 1024;
    const unsigned sb0 = (unsigned)B2_OFF + nt2 * (KS2 * 5120);
    ld.sa = (unsigned)A2_OFF + mt2 * (KS2 * 4096); ld.sb = sb0; ld.lw = lw;
    WAIT_V(12); BAR();
    if (lw == 0) {
#pragma unroll
      for (int i = 0; i < 4; ++i) { ld8_dev(pf[i], sy + SY_FB + 64 * i + lane); ld8_dev(pb[i], sy + SY_BASE + 64 * i + lane); }
    }
    WAIT_V(0);
    if (lw == 0) {
      bool ok = true;
#pragma unroll
      for (int i = 0; i < 4; ++i) {
        PIN(pf[i]); PIN(pb[i]); ok &= pf[i] == pb[i] + 1;
        *(LAS u64*)(uintptr_t)(K1_BASES + i * 512 + lane * 8) = pb[i];
      }
      if (!__all(ok)) {
#pragma unroll 1
        for (int i = 0; i < SPIN_CAP; ++i) { if (fb_up(sy, lane)) break; __builtin_amdgcn_s_sleep(POLL_SLEEP); }
      }
      WAIT_L0();
    }
    BAR();
    ACQUIRE();
    if (lw == 1 || lw == 2)
      asm volatile("s_mov_b32 m0, %0\n\ts_nop 0\n\tbuffer_load_dword %1, %2, %3 offen sc1 lds"
                   :: "s"(K1_G + (lw - 1) * 256), "v"(lane * 4), "s"(ld.rs), "s"((unsigned)G_OFF + (m0 + (lw - 1) * 64) * 4) : "memory");
    ld.load_b<0>(); ld.load_b<1>(); ld.load_b<2>();
    WAIT_V(15); BAR();
    ld.load_b<3>(); ld.load_b<4>();
    STAMP(ws, 0, 2);
    auto stage_b = [&](int n) {
      const int ks = ks_front(lw, n);
#pragma unroll
      for (int j = 0; j < 5; ++j) dma16_dev(lw * P2_REG + n * 5120 + j * 1024, ld.vo, ld.rs, sb0 + ks * 5120 + j * 1024);
    };
    constexpr int THIN = 5;
    stage_b(0); stage_b(1);
    wait_v<THIN>(); stage_b(2); wait_v<THIN>(); stage_b(3); wait_v<THIN>(); stage_b(4); wait_v<THIN>();
    const int Lp = (mt2 >> 1) * 16 + 8 + ((lane >> 1) & 7);
    const gu64* fm = sy + SY_FE + Lp * 4 + (mt2 & 1) * 2 + (lane & 1);
    stage_b(5); wait_v<THIN>(); stage_b(6);
    if (lw == 0) { ld8_dev(pf[0], fm); ld8_dev(pb[0], sy + SY_BASE + Lp); }
    acc_zero(ld.acc);
    WAIT_V(0);
    lds_post(4 + lw, 1u, lane);
    STAMP(ws, 0, 3);
    if (lw == 0) {
      PIN(pf[0]); PIN(pb[0]);
#pragma unroll 1
      for (int k = 0; k < SPIN_CAP && !__all(pf[0] == pb[0] + 1); ++k) { __builtin_amdgcn_s_sleep(POLL_SLEEP); pf[0] = sy_ld(fm); }
      lds_post(0, 1u, lane);
    } else lds_wait(0, 1u);
    ACQUIRE();
    STAMP(ws, 0, 4);
    ld.run();
    STAMP(ws, 0, 5);
    lds_wait(8 + lw, 1u);
    unsigned rr = lw * P2_REG + lane * 16;
    OPAQUE(rr);
#pragma unroll
    for (int i = 0; i < 4; ++i)
#pragma unroll
      for (int j = 0; j < 5; ++j)
        if (i > 0 || j < 2) *(LAS f32x4*)(uintptr_t)(rr + (i == 0 ? 18 + j : 15 + i * 5 + j) * 1024) = ld.acc[i][j];
    WAIT_L0();
    BAR();
    f32x4 r[3];
#pragma unroll
    for (int j = 2; j < 5; ++j) r[j - 2] = ld.acc[0][j] + *(const LAS f32x4*)(uintptr_t)(rr + (j - 2) * 1024);
#pragma unroll
    for (int d = 1; d < 4; ++d) {
      unsigned rq = ((lw - d) & 3) * P2_REG + lane * 16;
      OPAQUE(rq);
#pragma unroll
      for (int j = 2; j < 5; ++j)
        r[j - 2] += *(const LAS f32x4*)(uintptr_t)(rq + (d * 5 + j - 2) * 1024) + *(const LAS f32x4*)(uintptr_t)(rq + (15 + d * 5 + j) * 1024);
    }
    unsigned* op = (unsigned*)(out + (size_t)(mt2 * 64 + lw * 16 + (lane & 15)) * D + nt2 * 80 + (lane >> 4) * 20);
    *(u32x4_a8*)(op + 4) = u32x4{pack2(r[0][0], r[0][1]), pack2(r[0][2], r[0][3]), pack2(r[1][0], r[1][1]), pack2(r[1][2], r[1][3])};
    *(u32x2*)(op + 8) = u32x2{pack2(r[2][0], r[2][1]), pack2(r[2][2], r[2][3])};
    STAMP(ws, 0, 6);
    return;
  }

  const unsigned aep = K1_BASES + L * 8;
  if (threadIdx.x < 16) *(LAS unsigned*)(uintptr_t)(K1_NEWS + threadIdx.x * 4) = 0u;
  u32x2 bias[7];
  if (!attn) {
    const u32x2* bp = (const u32x2*)(b1 + (lane >> 4) * 4);
#pragma unroll
    for (int n = 0; n < 7; ++n) bias[n] = bp[min(nb0 + (n < 6 ? (wave & 1) * 8 + n : 6 + (wave & 1)), 107) * 4];
  }
  if (attn) {
    const int tok = m0 + nt * 16 + wave * 4 + (lane >> 4);
    const u32x4* gp = (const u32x4*)gate_w + (lane & 15);
    const u32x4* xr = (const u32x4*)(x + (size_t)tok * D) + (lane & 15);
    float sg = 0.f;
#pragma unroll
    for (int j = 0; j < 5; ++j) {
      const u32x4 a = xr[16 * j], w = gp[16 * j];
#pragma unroll
      for (int e = 0; e < 4; ++e) { sg = fmaf(bf_lo(a[e]), bf_lo(w[e]), sg); sg = fmaf(bf_hi(a[e]), bf_hi(w[e]), sg); }
    }
#pragma unroll
    for (int o = 8; o >= 1; o >>= 1) sg += __shfl_xor(sg, o, 64);
    if ((lane & 15) == 0)
      __hip_atomic_store((__attribute__((address_space(1))) float*)(ws + G_OFF) + tok, 1.f / (1.f + __expf(-sg)),
                         __ATOMIC_RELAXED, __HIP_MEMORY_SCOPE_AGENT);
  }
  if (!attn) {
#pragma unroll
    for (int n = 0; n < 7; ++n) PIN(bias[n]);
  }
  if (!attn) {
    const int wg = b & 127;
    const int pw = wg * 4 + wave;
    const int li = pw * 28 + (lane < 28 ? lane : 0);
    dma_touch(K1_PF, (unsigned)((li / 7) * (D * 2) + (3 + li % 7) * 128), make_rsrc(x, S_TOK * D * 2), 0);
    {
      const int q = ((idx & 15) * 4 + wave) * 63 + (lane < 63 ? lane : 0), row = q / 9, sl = q - row * 9;
      dma_touch(K1_PF, (unsigned)(row * (3 * D * 2) + (sl / 3) * (D * 2) + (sl % 3) * 128 + (xcd >> 1) * 320), make_rsrc(wqkv, D * 3 * D * 2), 192 * 3 * D * 2);
    }
    dma_touch(K1_PF, (unsigned)((pw * 24 + (lane < 24 ? lane : 0)) * 128), make_rsrc(w1, D * F * 2), 192 * F * 2);
  }
  WAIT_L0();
  if (attn) {
    AttnPacked pk;
    if (wave == 0) attn_main<0>(lane, pk, ws);
    else if (wave == 1) attn_main<1>(lane, pk, ws);
    else if (wave == 2) attn_main<2>(lane, pk, ws);
    else attn_main<3>(lane, pk, ws);
    attn_epilogue(lane, wave, mt, nt, pk, ws);
  } else {
    mlp_role(lane, wave, mt, nb0, bias, ws, sy + SY_FE + L * 4 + wave, aep);
  }
  const unsigned ep_lo = __builtin_amdgcn_readfirstlane(*(const LAS unsigned*)(uintptr_t)aep);
  const unsigned ep_hi = __builtin_amdgcn_readfirstlane(*(const LAS unsigned*)(uintptr_t)(aep + 4));
  const u64 epoch = (((u64)ep_hi << 32) | ep_lo) + 1;
  WAIT_V(0);
  if (lane == 0) sy_st(sy + SY_FA + L * 4 + wave, epoch);
  STAMP(ws, 0, 3);

  int ln = lane;
  OPAQUE(ln);
  const u32x4 rs = make_rsrc(ws, (unsigned)SY_OFF + 32768);
  const unsigned sa = (unsigned)A2_OFF + mt2 * (KS2 * 4096);
  unsigned va[4];
#pragma unroll
  for (int i = 0; i < 4; ++i) va[i] = ln * 16 + ((i + wave) & 3) * 1024;
  u32x4 fa[4][4], eb[4][5];
  const int La = (mt2 >> 1) * 16 + (ln < 32 ? (ln >> 2) : 8 + ((ln >> 1) & 7));
  const gu64* fat = sy + SY_FA + La * 4 + (ln < 32 ? (ln & 3) : (mt2 & 1) * 2 + (ln & 1));
  u64 pp[4];
  u64 up = 0;
  const unsigned sbb = (unsigned)B2_OFF + nt2 * (KS2 * 5120), vo = ln * 16;
#define P2_B(n) { _Pragma("unroll") for (int j = 0; j < 5; ++j) ld16_dev(eb[n - 7][j], vo, rs, sbb + ks_front(wave, n) * 5120 + j * 1024); }
  P2_B(7) P2_B(8) P2_B(9)
  f32x4 acc[4][5];
  acc_zero(acc);
  unsigned rb = wave * P2_REG + ln * 16;
  OPAQUE(rb);
#define P2_A(n) { _Pragma("unroll") for (int i = 0; i < 4; ++i) ld16_dev(fa[(n) & 3][i], va[i], rs, sa + ks_front(wave, n) * 4096); }
#define P2_STEP(n, CNT) { u32x4 fb[5]; _Pragma("unroll") for (int j = 0; j < 5; ++j) fb[j] = lds_read128(rb + n * 5120 + j * 1024); \
    wait_pin<CNT>(fa[(n) & 3]); mma20(acc, fa[(n) & 3], fb); SCHED(); }
#define P2_EXTRA(n, CNT) { wait_pin<CNT>(fa[(n) & 3]); _Pragma("unroll") for (int j = 0; j < 5; ++j) PIN(eb[n - 7][j]); \
    mma20(acc, fa[(n) & 3], eb[n - 7]); SCHED(); }
  lds_wait(4 + wave, 1u);
  lds_wait(0, 1u);
  ACQUIRE();
  if (wave == 0) { ld8_dev(pp[0], fat); ld8_dev(pp[1], sy + SY_BASE + La); }
  P2_A(0) P2_A(1) P2_A(2)
  if (wave == 0) {
    WAIT_V(12);
    PIN(pp[0]); PIN(pp[1]);
    up = __ballot(pp[0] == pp[1] + 1);
#pragma unroll 1
    for (int k = 0; k < SPIN_CAP && (up >> 32) != 0xffffffffull; ++k) { __builtin_amdgcn_s_sleep(POLL_SLEEP); up = __ballot(sy_ld(fat) == pp[1] + 1); }
    lds_post(0, up == ~0ull ? 3u : 2u, ln);
  }
  STAMP(ws, 0, 4);
  P2_STEP(0, 8)
  lds_wait(0, 2u);
  ACQUIRE();
  if (wave == 0) ld8_dev(pp[0], fat);
  P2_A(3) P2_A(4)
  P2_STEP(1, 12) P2_A(5)
  P2_STEP(2, 12)
  if (wave == 0) {
    PIN(pp[0]);
    up |= __ballot(pp[0] == pp[1] + 1);
#pragma unroll 1
    for (int k = 0; k < SPIN_CAP && up != ~0ull; ++k) { up = __ballot(sy_ld(fat) == pp[1] + 1); if (up != ~0ull) __builtin_amdgcn_s_sleep(POLL_SLEEP); }
    lds_post(0, 3u, ln);
    if (ln == 0) sy_st(sy + SY_PASS + L, epoch);
  }
  P2_A(6)
  P2_STEP(3, 12)
  lds_wait(0, 3u);
  ACQUIRE();
  P2_A(7)
  P2_STEP(4, 12) P2_A(8)
  P2_STEP(5, 12) P2_A(9)
  P2_STEP(6, 12) P2_B(10) P2_A(10)
  P2_EXTRA(7, 17) P2_EXTRA(8, 13) P2_EXTRA(9, 9) P2_EXTRA(10, 0)
  ACC_OUT();
#undef P2_A
#undef P2_B
#undef P2_STEP
#undef P2_EXTRA
  if (wave == 0) {
#pragma unroll
    for (int i = 0; i < 4; ++i) ld8_buf(pp[i], ln * 8, rs, (unsigned)SY_OFF + (SY_PASS + 64 * i) * 8);
  }
#pragma unroll
  for (int i = 0; i < 4; ++i)
#pragma unroll
    for (int j = 0; j < 5; ++j)
      if (i > 0 || j >= 2) *(LAS f32x4*)(uintptr_t)(rb + (i * 5 + j - 2) * 1024) = acc[i][j];
  WAIT_L0();
  lds_post(8 + wave, 1u, ln);
  STAMP(ws, 0, 5);
  BAR();
  {
    f32x4 r[2];
#pragma unroll
    for (int j = 0; j < 2; ++j) r[j] = acc[0][j] + *(const LAS f32x4*)(uintptr_t)(rb + (18 + j) * 1024);
#pragma unroll
    for (int d = 1; d < 4; ++d) {
      unsigned rq = ((wave - d) & 3) * P2_REG + ln * 16;
      OPAQUE(rq);
#pragma unroll
      for (int j = 0; j < 2; ++j)
        r[j] += *(const LAS f32x4*)(uintptr_t)(rq + (d * 5 + j - 2) * 1024) + *(const LAS f32x4*)(uintptr_t)(rq + (15 + d * 5 + j) * 1024);
    }
    if (wave == 0) WAIT_V(0);
    unsigned* op = (unsigned*)(out + (size_t)(mt2 * 64 + wave * 16 + (ln & 15)) * D + nt2 * 80 + (ln >> 4) * 20);
    *(u32x4_a8*)op = u32x4{pack2(r[0][0], r[0][1]), pack2(r[0][2], r[0][3]), pack2(r[1][0], r[1][1]), pack2(r[1][2], r[1][3])};
  }
  if (wave == 0) {
    u64 wb[4];
#pragma unroll
    for (int i = 0; i < 4; ++i) { PIN(pp[i]); wb[i] = *(const LAS u64*)(uintptr_t)(K1_BASES + i * 512 + ln * 8); }
#pragma unroll 1
    for (int k = 0; k < SPIN_CAP; ++k) {
      if (__all(pp[0] == wb[0] + 1 && pp[1] == wb[1] + 1 && pp[2] == wb[2] + 1 && pp[3] == wb[3] + 1)) break;
      __builtin_amdgcn_s_sleep(POLL_SLEEP);
#pragma unroll
      for (int i = 0; i < 4; ++i) pp[i] = sy_ld(sy + SY_PASS + 64 * i + ln);
    }
    if (ln == 0) sy_st(sy + SY_BASE + L, epoch);
  }
  STAMP(ws, 0, 6);
}

}

extern "C" void kernel_launch(void* const* d_in, const int* in_sizes, int n_in, void* d_out, int out_size, void* d_ws,
                              size_t ws_size, hipStream_t stream) {
  const unsigned short* x = (const unsigned short*)d_in[0];
  const unsigned short* wqkv = (const unsigned short*)d_in[1];
  const unsigned short* wo = (const unsigned short*)d_in[2];
  const unsigned short* w1 = (const unsigned short*)d_in[4];
  const unsigned short* b1 = (const unsigned short*)d_in[5];
  const unsigned short* w2 = (const unsigned short*)d_in[6];
  const unsigned short* gate_w = (const unsigned short*)d_in[7];
  (void)hipFuncSetAttribute((const void*)fused_kernel, hipFuncAttributeMaxDynamicSharedMemorySize, K1_LDS);
  fused_kernel<<<256, 512, K1_LDS, stream>>>(x, wqkv, wo, w1, b1, w2, gate_w, (unsigned char*)d_ws, (unsigned short*)d_out);
}
```

```cpp
#include <hip/hip_runtime.h>
#include <hip/hip_bf16.h>
#include <stdint.h>

#define STAMP(ws, kern, i)
#define CODE_BYTES 34816

namespace {

typedef __attribute__((ext_vector_type(4))) short s16x4;
typedef __attribute__((ext_vector_type(8))) __bf16 bf16x8;
typedef __attribute__((ext_vector_type(2))) __bf16 bf16x2;
typedef __attribute__((ext_vector_type(4))) float f32x4;
typedef __attribute__((ext_vector_type(2))) float f32x2;
typedef __attribute__((ext_vector_type(4))) unsigned u32x4;
typedef __attribute__((ext_vector_type(2))) unsigned u32x2;
#define LAS __attribute__((address_space(3)))

constexpr int S_TOK = 2048, D = 640, F = 1728;
constexpr int KS2 = 74;
constexpr size_t A2_BYTES = (size_t)128 * KS2 * 1024;
constexpr size_t B2_BYTES = (size_t)40 * KS2 * 1024;
constexpr size_t A2_OFF = 0, B2_OFF = A2_OFF + A2_BYTES, G_OFF = B2_OFF + B2_BYTES;
constexpr size_t SY_OFF = G_OFF + 8192;
constexpr int SY_BASE = 0, SY_PASS = 256, SY_FB = 512, SY_FA = 1024, SY_FE = 2048;

constexpr int X_BYTES = 144 * 128;
constexpr int W_ROW = 480;
constexpr int W_BYTES = 64 * W_ROW;
constexpr int STAGE = X_BYTES + W_BYTES;
constexpr int NSTAGE = 3;
constexpr int EX_OFF = NSTAGE * STAGE;
constexpr int EX_Q = EX_OFF, EX_K = EX_Q + 8 * 512, EX_V = EX_K + 9 * 512;
constexpr int K1_PF = EX_OFF + 3 * 5120;
constexpr int K1_BASES = EX_V + 9 * 512;
static_assert(K1_BASES + 2048 <= K1_PF, "LDS map");
constexpr int K1_NEWS = K1_PF + 256;
constexpr int K1_G = K1_NEWS + 64;
constexpr int K1_LDS = K1_G + 512;
constexpr int NT1 = 10;

__device__ __forceinline__ unsigned pack2(float a, float b) {
  f32x2 v = {a, b};
  bf16x2 r = __builtin_convertvector(v, bf16x2);
  return __builtin_bit_cast(unsigned, r);
}
__device__ __forceinline__ float bf_lo(unsigned u) { return __builtin_bit_cast(float, u << 16); }
__device__ __forceinline__ float bf_hi(unsigned u) { return __builtin_bit_cast(float, u & 0xffff0000u); }
__device__ __forceinline__ f32x4 mfma16(u32x4 a, u32x4 b, f32x4 c) {
  return __builtin_amdgcn_mfma_f32_16x16x32_bf16(__builtin_bit_cast(bf16x8, a), __builtin_bit_cast(bf16x8, b), c, 0, 0, 0);
}
__device__ __forceinline__ u32x4 lds_read128(unsigned addr) { return *(const LAS u32x4*)(uintptr_t)addr; }
__device__ __forceinline__ u32x2 lds_read64(unsigned addr) { return *(const LAS u32x2*)(uintptr_t)addr; }
__device__ __forceinline__ u32x2 lds_tr(unsigned addr) {
  s16x4 v = __builtin_amdgcn_ds_read_tr16_b64_v4i16((LAS s16x4*)(uintptr_t)addr);
  return __builtin_bit_cast(u32x2, v);
}
__device__ __forceinline__ void dma16(unsigned ldsaddr, unsigned voff, u32x4 rsrc, unsigned soff) {
  asm volatile("s_mov_b32 m0, %0\n\ts_nop 0\n\tbuffer_load_dwordx4 %1, %2, %3 offen lds"
               :: "s"(ldsaddr), "v"(voff), "s"(rsrc), "s"(soff) : "memory");
}
__device__ __forceinline__ void dma16_dev(unsigned ldsaddr, unsigned voff, u32x4 rsrc, unsigned soff) {
  asm volatile("s_mov_b32 m0, %0\n\ts_nop 0\n\tbuffer_load_dwordx4 %1, %2, %3 offen sc1 lds"
               :: "s"(ldsaddr), "v"(voff), "s"(rsrc), "s"(soff) : "memory");
}
__device__ __forceinline__ void dma_touch(unsigned ldsaddr, unsigned voff, u32x4 rsrc, unsigned soff) {
  asm volatile("s_mov_b32 m0, %0\n\ts_nop 0\n\tbuffer_load_dword %1, %2, %3 offen lds"
               :: "s"(ldsaddr), "v"(voff), "s"(rsrc), "s"(soff) : "memory");
}
__device__ __forceinline__ void st16(void* p, u32x4 v) {
  asm volatile("global_store_dwordx4 %0, %1, off sc1\n\ts_nop 1" :: "v"(p), "v"(v) : "memory");
}
constexpr int ST_AUX = 16;
typedef __amdgpu_buffer_rsrc_t rsrc_t;
__device__ __forceinline__ rsrc_t out_rsrc(void* p, unsigned bytes) { return __builtin_amdgcn_make_buffer_rsrc(p, 0, bytes, 0x00020000); }
template <int AUX> __device__ __forceinline__ void bst16(rsrc_t r, unsigned voff, u32x4 v) { __builtin_amdgcn_raw_buffer_store_b128(v, r, voff, 0, AUX); }
template <int AUX> __device__ __forceinline__ void bst8(rsrc_t r, unsigned voff, unsigned soff, u32x2 v) { __builtin_amdgcn_raw_buffer_store_b64(v, r, voff, soff, AUX); }
#define WAIT_V(n) asm volatile("s_waitcnt vmcnt(" #n ")" ::: "memory")
#define WAIT_L0() __builtin_amdgcn_s_waitcnt(0xC07F)
#define OPAQUE(x) asm volatile("" : "+v"(x))
#define BAR() do { asm volatile("" ::: "memory"); __builtin_amdgcn_s_barrier(); asm volatile("" ::: "memory"); } while (0)
#define SCHED() __builtin_amdgcn_sched_barrier(0)

__device__ __forceinline__ u32x4 make_rsrc(const void* p, unsigned bytes) {
  uint64_t a = (uint64_t)p;
  u32x4 r;
  r.x = __builtin_amdgcn_readfirstlane((unsigned)a);
  r.y = __builtin_amdgcn_readfirstlane((unsigned)(a >> 32) & 0xffffu);
  r.z = bytes;
  r.w = 0x00020000u;
  return r;
}

__device__ __forceinline__ int swap23(int k) { return (k & ~12) | ((k & 4) << 1) | ((k & 8) >> 1); }

struct Stager {
  unsigned vx, vw[7], v4;
  u32x4 rx, rw, r4;
  unsigned sx, sw, s4;
  unsigned wstep, step4;
  unsigned lx, lw, l4;
};
__device__ __forceinline__ void stage_x(const Stager& st, unsigned sb) {
#pragma unroll
  for (int p = 0; p < 4; ++p) dma16(sb + st.lx + p * 4096, st.vx, st.rx, st.sx + p * (32 * D * 2));
}
__device__ __forceinline__ void stage_w(const Stager& st, unsigned sb) {
#pragma unroll
  for (int j = 0; j < 7; ++j) dma16(sb + st.lw + j * 4096, st.vw[j], st.rw, st.sw);
  dma16(sb + st.l4, st.v4, st.r4, st.s4);
}
__device__ __forceinline__ void stage_next(Stager& st) { st.sx += 128u; st.sw += st.wstep; st.s4 += st.step4; }
__device__ __forceinline__ unsigned w_src(int wi, int lane, unsigned rowbytes, unsigned secstride, int chmax) {
  unsigned ci = wi * 64 + lane;
  unsigned pos = (ci * 2185u) >> 16, ch = ci - pos * 30u;
  unsigned sec = (ch * 205u) >> 11;
  unsigned k = swap23(pos);
  ch = ch < (unsigned)chmax ? ch : (unsigned)chmax;
  return k * rowbytes + ch * 16u + sec * secstride;
}

struct AttnAcc { f32x4 q[8], k[9], v[9], e[9]; };
__device__ __forceinline__ u32x4 cat(u32x2 a, u32x2 b) { return u32x4{a.x, a.y, b.x, b.y}; }

template <int W>
struct AttnOps {
  static constexpr int NE = W < 3 ? 1 : 3;
  struct Frags { u32x4 x[9]; u32x2 wl[3 + NE], wh[3 + NE]; };
  AttnAcc a;
  unsigned xa, wa, we;
  static constexpr int n_e(int rb) { return W == 0 ? rb < 7 : (W < 3 ? (rb == 8 || rb < 6) : (rb == 6 ? 2 : (rb == 7 ? 3 : 0))); }
  static constexpr int per_rb(int rb) { return 2 + (rb < 8 ? 1 : 0) + n_e(rb); }
  static constexpr int first(int o) { int c = 0; for (int i = 0; i < o; ++i) c += per_rb(i == 0 ? 8 : i - 1); return c; }
  static constexpr int pos_of(int I) { int o = 0; while (first(o + 1) <= I) ++o; return o; }
  static constexpr int NM = first(9);
  static constexpr int NR = 15 + 2 * NE;
  template <int I> __device__ __forceinline__ void mma(const Frags& f) {
    constexpr int o = pos_of(I), rb = o == 0 ? 8 : o - 1, j = I - first(o);
    if constexpr (j == 0) a.k[rb] = mfma16(cat(f.wl[1], f.wh[1]), f.x[rb], a.k[rb]);
    else if constexpr (j == 1) a.v[rb] = mfma16(f.x[rb], cat(f.wl[2], f.wh[2]), a.v[rb]);
    else if constexpr (j < 2 + n_e(rb)) {
      if constexpr (W == 2) a.e[rb] = mfma16(f.x[rb], cat(f.wl[3], f.wh[3]), a.e[rb]);
      else if constexpr (W < 2) a.e[rb] = mfma16(cat(f.wl[3], f.wh[3]), f.x[rb], a.e[rb]);
      else if constexpr (j == 2) a.e[rb - 5] = mfma16(cat(f.wl[4], f.wh[4]), f.x[rb], a.e[rb - 5]);
      else if constexpr (j == 3) a.e[rb - 3] = mfma16(f.x[rb], cat(f.wl[5], f.wh[5]), a.e[rb - 3]);
      else a.e[0] = mfma16(cat(f.wl[3], f.wh[3]), f.x[rb], a.e[0]);
    } else a.q[rb] = mfma16(cat(f.wl[0], f.wh[0]), f.x[rb], a.q[rb]);
  }
  __device__ __forceinline__ void init() {
#pragma unroll
    for (int i = 0; i < 9; ++i) {
      if (i < 8) a.q[i] = f32x4{0.f, 0.f, 0.f, 0.f};
      a.k[i] = f32x4{0.f, 0.f, 0.f, 0.f}; a.v[i] = f32x4{0.f, 0.f, 0.f, 0.f}; a.e[i] = f32x4{0.f, 0.f, 0.f, 0.f};
    }
  }
  unsigned x, w, e;
  __device__ __forceinline__ void bases(unsigned sb, int kk) {
    x = (xa ^ (kk * 64)) + sb; w = wa + sb + kk * 32 * W_ROW; e = we + sb + kk * 32 * W_ROW;
    OPAQUE(x); OPAQUE(w); OPAQUE(e);
  }
  template <int I> __device__ __forceinline__ void read(Frags& f) {
    if constexpr (I == 0) f.wl[1] = lds_tr(w + 160);
    if constexpr (I == 1) f.wh[1] = lds_tr(w + 160 + 8 * W_ROW);
    if constexpr (I == 2) f.x[8] = lds_read128(x + 8 * 2048);
    if constexpr (I == 3) f.wl[2] = lds_tr(w + 320);
    if constexpr (I == 4) f.wh[2] = lds_tr(w + 320 + 8 * W_ROW);
    if constexpr (I == 5) f.wl[3] = lds_tr(e);
    if constexpr (I == 6) f.wh[3] = lds_tr(e + 8 * W_ROW);
    if constexpr (I == 7) f.x[0] = lds_read128(x);
    if constexpr (I == 8) f.wl[0] = lds_tr(w);
    if constexpr (I == 9) f.wh[0] = lds_tr(w + 8 * W_ROW);
    if constexpr (I >= 10 && I < 17) f.x[I - 9] = lds_read128(x + (I - 9) * 2048);
    if constexpr (I >= 17) {
      constexpr int n = 4 + (I - 17) / 2;
      if constexpr ((I - 17) % 2 == 0) f.wl[n] = lds_tr(e + 160 * (n - 3)); else f.wh[n] = lds_tr(e + 160 * (n - 3) + 8 * W_ROW);
    }
  }
};

struct MlpAcc { f32x4 c[4][7]; };
struct MlpOps {
  struct Frags { u32x4 x[4]; u32x2 wl[7], wh[7]; };
  MlpAcc a;
  unsigned xa, wa;
  static constexpr int NM = 28, NR = 18;
  template <int I> __device__ __forceinline__ void mma(const Frags& f) {
    constexpr int n = I / 4, r = I % 4;
    a.c[r][n] = mfma16(cat(f.wl[n], f.wh[n]), f.x[r], a.c[r][n]);
  }
  u32x2 bias[7];
  __device__ __forceinline__ void init() {
#pragma unroll
    for (int n = 0; n < 7; ++n) {
      f32x4 b = {bf_lo(bias[n].x), bf_hi(bias[n].x), bf_lo(bias[n].y), bf_hi(bias[n].y)};
#pragma unroll
      for (int r = 0; r < 4; ++r) a.c[r][n] = b;
    }
  }
  unsigned x, w, w6, d6;
  __device__ __forceinline__ void bases(unsigned sb, int kk) {
    x = (xa ^ (kk * 64)) + sb; w = wa + sb + kk * 32 * W_ROW; w6 = w + d6;
    OPAQUE(x); OPAQUE(w); OPAQUE(w6);
  }
  template <int I> __device__ __forceinline__ void read(Frags& f) {
    if constexpr (I < 2) { if constexpr (I == 0) f.wl[0] = lds_tr(w); else f.wh[0] = lds_tr(w + 8 * W_ROW); }
    else if constexpr (I < 6) f.x[I - 2] = lds_read128(x + (I - 2) * 2048);
    else {
      constexpr int n = 1 + (I - 6) / 2;
      const unsigned a = n < 6 ? w + 32 * n : w6;
      if constexpr ((I & 1) == 0) f.wl[n] = lds_tr(a); else f.wh[n] = lds_tr(a + 8 * W_ROW);
    }
  }
};

template <class OPS, int I>
__device__ __forceinline__ void read_all(OPS& o, typename OPS::Frags& f) {
  if constexpr (I < OPS::NR) { o.template read<I>(f); read_all<OPS, I + 1>(o, f); }
}
template <class OPS, int I>
__device__ __forceinline__ void half_step(OPS& o, const typename OPS::Frags& fc, typename OPS::Frags& fn) {
  if constexpr (I < OPS::NM) {
    o.template mma<I>(fc);
    constexpr int span = OPS::NM - 6;
    static_assert(span >= OPS::NR, "one read per gap");
    constexpr int j = (I * OPS::NR + span - 1) / span;
    if constexpr (j < OPS::NR && j * span / OPS::NR == I) { SCHED(); o.template read<j>(fn); }
    SCHED();
    half_step<OPS, I + 1>(o, fc, fn);
  }
}

template <class OPS>
__device__ __forceinline__ void k_loop(OPS& o) {
  typename OPS::Frags f0, f1;
  o.init();
  o.bases(0, 0);
  BAR();
  read_all<OPS, 0>(o, f0);
  unsigned sb = 0;
#pragma unroll 1
  for (int t = 0; t < NT1; ++t) {
    unsigned sn = sb + STAGE; if (sn == NSTAGE * STAGE) sn = 0;
    o.bases(sb, 1);
    WAIT_L0();
    SCHED();
    half_step<OPS, 0>(o, f0, f1);
    o.bases(sn, 0);
    WAIT_L0();
    if (t == 2) WAIT_V(0);
    BAR();
    SCHED();
    half_step<OPS, 0>(o, f1, f0);
    sb = sn;
  }
}

__device__ __forceinline__ float fmx(float a, float b) { return __builtin_elementwise_maximum(a, b); }
__device__ __forceinline__ void swap32(float& a, float& b) {
  auto r = __builtin_amdgcn_permlane32_swap(__builtin_bit_cast(unsigned, a), __builtin_bit_cast(unsigned, b), false, false);
  unsigned r0 = r[0], r1 = r[1];
  a = __builtin_bit_cast(float, r0); b = __builtin_bit_cast(float, r1);
}
__device__ __forceinline__ void swap16(float& a, float& b) {
  auto r = __builtin_amdgcn_permlane16_swap(__builtin_bit_cast(unsigned, a), __builtin_bit_cast(unsigned, b), false, false);
  unsigned r0 = r[0], r1 = r[1];
  a = __builtin_bit_cast(float, r0); b = __builtin_bit_cast(float, r1);
}
__device__ __forceinline__ u32x4 widen_pair(u32x2 a, u32x2 b) {
  auto x = __builtin_amdgcn_permlane16_swap(a.x, b.x, false, false);
  auto y = __builtin_amdgcn_permlane16_swap(a.y, b.y, false, false);
  unsigned x0 = x[0], x1 = x[1], y0 = y[0], y1 = y[1];
  return u32x4{x0, y0, x1, y1};
}
template <bool MAX>
__device__ __forceinline__ float reduce4(float x0, float x1, float x2, float x3) {
  swap32(x0, x1); swap32(x2, x3);
  float y01 = MAX ? fmx(x0, x1) : x0 + x1, y23 = MAX ? fmx(x2, x3) : x2 + x3;
  swap16(y01, y23);
  return MAX ? fmx(y01, y23) : y01 + y23;
}
__device__ __forceinline__ void bcast4(float z, float& x0, float& x1, float& x2, float& x3) {
  float u = z, w = z;
  swap16(u, w);
  x0 = u; x1 = u; swap32(x0, x1);
  x2 = w; x3 = w; swap32(x2, x3);
}

constexpr int SPIN_CAP = 1 << 16;
__device__ __forceinline__ void lds_wait(int word, unsigned atleast) {
  volatile LAS unsigned* p = (volatile LAS unsigned*)(uintptr_t)(K1_NEWS + word * 4);
#pragma unroll 1
  for (int i = 0; i < 16 * SPIN_CAP; ++i) { if (__builtin_amdgcn_readfirstlane(*p) >= atleast) break; __builtin_amdgcn_s_sleep(1); }
  asm volatile("" ::: "memory");
}
__device__ __forceinline__ void lds_post(int word, unsigned v, int lane) {
  asm volatile("" ::: "memory");
  if (lane == 0) *(volatile LAS unsigned*)(uintptr_t)(K1_NEWS + word * 4) = v;
}

struct AttnPacked { u32x2 q[8], k[9], v[9]; };

template <int W>
__device__ __forceinline__ void attn_main(int lane, AttnPacked& pk, unsigned char* __restrict__ ws) {
  OPAQUE(lane);
  const int c = lane & 15, g = lane >> 4, q4 = (lane >> 2) & 3, p4 = lane & 3;
  AttnOps<W> o;
  o.xa = c * 128 + ((g ^ ((c >> 1) & 7)) * 16);
  const unsigned wrow = X_BYTES + ((g >> 1) * 16 + (g & 1) * 4 + q4) * W_ROW;
  o.wa = wrow + 40 * W + 8 * p4;
  o.we = wrow + 160 * (W < 3 ? W : 0) + 40 * p4 + 32;
  STAMP(ws, 0, 1);
  k_loop(o);
  STAMP(ws, 0, 2);
  OPAQUE(lane);
#pragma unroll
  for (int i = 0; i < 9; ++i) {
    constexpr int base = W == 0 ? EX_Q : (W == 1 ? EX_K : EX_V);
    constexpr int w3[5] = {EX_Q + 7 * 512, EX_K + 6 * 512, EX_K + 7 * 512, EX_V + 6 * 512, EX_V + 7 * 512};
    if (W < 3 ? AttnOps<W>::n_e(i) != 0 : i < 5) {
      u32x2 v = {pack2(o.a.e[i][0], o.a.e[i][1]), pack2(o.a.e[i][2], o.a.e[i][3])};
      *(LAS u32x2*)(uintptr_t)((W < 3 ? base + i * 512 : w3[i < 5 ? i : 0]) + lane * 8) = v;
    }
  }
#pragma unroll
  for (int i = 0; i < 9; ++i) {
    if (i < 8) pk.q[i] = u32x2{pack2(o.a.q[i][0], o.a.q[i][1]), pack2(o.a.q[i][2], o.a.q[i][3])};
    pk.k[i] = u32x2{pack2(o.a.k[i][0], o.a.k[i][1]), pack2(o.a.k[i][2], o.a.k[i][3])};
    pk.v[i] = u32x2{pack2(o.a.v[i][0], o.a.v[i][1]), pack2(o.a.v[i][2], o.a.v[i][3])};
  }
}

__device__ __forceinline__ void attn_epilogue(int lane, int W, int mt, int hg, AttnPacked& pk, unsigned char* __restrict__ ws) {
  OPAQUE(lane);
  const int c = lane & 15, g = lane >> 4;
  f32x4 mprev, mcur, mfirst;
#pragma unroll
  for (int r = 0; r < 4; ++r) {
    int dcur = c - 4 * g - r, dprev = dcur + 16;
    mcur[r] = (dcur >= 0 && (dcur & 1) == 0) ? 0.f : -1e30f;
    mprev[r] = (dprev <= 14 && (dprev & 1) == 0) ? 0.f : -1e30f;
    mfirst[r] = mt == 0 ? -1e30f : mprev[r];
  }
  const float c2 = 0.22360679774997896f * 1.4426950408889634f;
  const unsigned sel_lo = (c & 1) ? 0x05040c0cu : 0x0c0c0504u, sel_hi = (c & 1) ? 0x07060c0cu : 0x0c0c0706u;
  unsigned exa = EX_OFF + W * 128 + c * 8 + g * 2, exv = EX_V + (g * 16 + W * 4 + (c & 3)) * 8;
  unsigned exg = K1_G + c * 4;
  OPAQUE(exa); OPAQUE(exv); OPAQUE(exg);
  asm volatile("" ::: "memory");
  WAIT_L0();
  if (lane == 0) __hip_atomic_fetch_add((LAS unsigned*)(uintptr_t)(K1_NEWS + 4), 1u, __ATOMIC_RELAXED, __HIP_MEMORY_SCOPE_WORKGROUP);
#pragma unroll
  for (int i = 0; i < 9; ++i) { if (i < 8) OPAQUE(pk.q[i]); OPAQUE(pk.k[i]); OPAQUE(pk.v[i]); }
  OPAQUE(mprev); OPAQUE(mcur); OPAQUE(mfirst);
  lds_wait(1, 4u);
  unsigned kx[9], qx[8]; u32x2 vx[9];
#pragma unroll
  for (int i = 0; i < 9; ++i) {
    kx[i] = *(const LAS unsigned short*)(uintptr_t)(exa + (EX_K - EX_OFF) + i * 512);
    if (i < 8) qx[i] = *(const LAS unsigned short*)(uintptr_t)(exa + (EX_Q - EX_OFF) + i * 512);
  }
#pragma unroll
  for (int i = 0; i < 9; ++i) vx[i] = lds_read64(exv + i * 512);
  float gt[8];
#pragma unroll
  for (int i = 0; i < 8; ++i) gt[i] = *(const LAS float*)(uintptr_t)(exg + i * 64);
  float t[8][4], m[8];
  f32x4 sp_[8], sc_[8];
#pragma unroll
  for (int i = 0; i < 8; ++i) {
    const int ip = i == 0 ? 8 : i - 1;
    const u32x4 qf = {pk.q[i].x, pk.q[i].y, qx[i], 0u};
    sp_[i] = mfma16(u32x4{pk.k[ip].x, pk.k[ip].y, kx[ip], 0u}, qf, i == 0 ? mfirst : mprev);
    sc_[i] = mfma16(u32x4{pk.k[i].x, pk.k[i].y, kx[i], 0u}, qf, mcur);
  }
  SCHED();
#pragma unroll
  for (int i = 0; i < 8; ++i) {
    const f32x4 sp = sp_[i], sc = sc_[i];
    t[i][0] = fmx(sp[0], sp[1]); t[i][1] = fmx(sp[2], sp[3]); t[i][2] = fmx(sc[0], sc[1]); t[i][3] = fmx(sc[2], sc[3]);
    m[i] = fmx(fmx(t[i][0], t[i][1]), fmx(t[i][2], t[i][3]));
  }
  bcast4(reduce4<true>(m[0], m[1], m[2], m[3]) * -c2, m[0], m[1], m[2], m[3]);
  bcast4(reduce4<true>(m[4], m[5], m[6], m[7]) * -c2, m[4], m[5], m[6], m[7]);
  float l[8];
#pragma unroll
  for (int i = 0; i < 8; ++i) {
#pragma unroll
    for (int r = 0; r < 4; ++r) t[i][r] = __builtin_amdgcn_exp2f(fmaf(t[i][r], c2, m[i]));
    l[i] = (t[i][0] + t[i][1]) + (t[i][2] + t[i][3]);
  }
  bcast4(__builtin_amdgcn_rcpf(reduce4<false>(l[0], l[1], l[2], l[3])), l[0], l[1], l[2], l[3]);
  bcast4(__builtin_amdgcn_rcpf(reduce4<false>(l[4], l[5], l[6], l[7])), l[4], l[5], l[6], l[7]);
  const int H = hg * 4 + W;
  const rsrc_t ro = out_rsrc(ws + A2_OFF, (unsigned)A2_BYTES);
  unsigned vm = (mt * 2 * KS2 + (H >> 1)) * 4096 + (H & 1) * 512 + (g & 1) * 1024 + (g >> 1) * 256 + c * 16;
  unsigned vx_ = (mt * 2 * KS2 + 16 + (hg >> 1)) * 4096 + (hg & 1) * 512 + g * 1024 + (W >> 1) * 256 + c * 16 + (W & 1) * 8;
  u32x4 pf[8];
#pragma unroll
  for (int i = 0; i < 8; ++i) {
    const float sc = l[i] * gt[i];
    const unsigned p01 = pack2(t[i][0] * sc, t[i][1] * sc), p23 = pack2(t[i][2] * sc, t[i][3] * sc);
    pf[i] = u32x4{__builtin_amdgcn_perm(p01, 0u, sel_lo), __builtin_amdgcn_perm(p01, 0u, sel_hi),
                  __builtin_amdgcn_perm(p23, 0u, sel_lo), __builtin_amdgcn_perm(p23, 0u, sel_hi)};
  }
  f32x4 om[8], ox[8];
#pragma unroll
  for (int i = 0; i < 8; ++i) {
    const int ip = i == 0 ? 8 : i - 1;
    const f32x4 z = {0.f, 0.f, 0.f, 0.f};
    om[i] = mfma16(cat(pk.v[ip], pk.v[i]), pf[i], z);
    ox[i] = mfma16(cat(vx[ip], vx[i]), pf[i], z);
  }
  SCHED();
  const bool g1 = g & 1, g2 = g & 2;
#pragma unroll
  for (int h = 0; h < 2; ++h) {
    u32x2 sm[4];
    unsigned ex[4], ey[4];
#pragma unroll
    for (int i = 0; i < 4; ++i) {
      sm[i] = u32x2{pack2(om[4 * h + i][0], om[4 * h + i][1]), pack2(om[4 * h + i][2], om[4 * h + i][3])};
      ex[i] = pack2(ox[4 * h + i][0], ox[4 * h + i][1]); ey[i] = pack2(ox[4 * h + i][2], ox[4 * h + i][3]);
    }
    bst16<ST_AUX>(ro, vm, widen_pair(sm[0], sm[1]));
    bst16<ST_AUX>(ro, vm + 2048, widen_pair(sm[2], sm[3]));
    unsigned x01 = g1 ? ex[1] : ex[0], x23 = g1 ? ex[3] : ex[2], y01 = g1 ? ey[1] : ey[0], y23 = g1 ? ey[3] : ey[2];
    OPAQUE(x01); OPAQUE(x23); OPAQUE(y01); OPAQUE(y23);
    bst8<ST_AUX>(ro, vx_, 0, u32x2{g2 ? x23 : x01, g2 ? y23 : y01});
    vm += KS2 * 4096; vx_ += KS2 * 4096;
  }
}

__device__ __forceinline__ u32x4 mish8(f32x4 h0, f32x4 h1, float s0, float s1) {
  const f32x2 h[4] = {{h0[0], h0[1]}, {h0[2], h0[3]}, {h1[0], h1[1]}, {h1[2], h1[3]}};
  f32x2 w[4];
#pragma unroll
  for (int i = 0; i < 4; ++i) w[i] = h[i] * 1.4426950408889634f;
  SCHED();
#pragma unroll
  for (int i = 0; i < 4; ++i) w[i] = f32x2{__builtin_amdgcn_exp2f(w[i].x), __builtin_amdgcn_exp2f(w[i].y)};
  SCHED();
#pragma unroll
  for (int i = 0; i < 4; ++i) w[i] = w[i] + 1.f;
  SCHED();
#pragma unroll
  for (int i = 0; i < 4; ++i) w[i] = __builtin_elementwise_fma(w[i], w[i], f32x2{1.f, 1.f});
  SCHED();
#pragma unroll
  for (int i = 0; i < 4; ++i) w[i] = f32x2{__builtin_amdgcn_rcpf(w[i].x), __builtin_amdgcn_rcpf(w[i].y)};
  SCHED();
#pragma unroll
  for (int i = 0; i < 4; ++i) { const float sc = i < 2 ? s0 : s1; w[i] = __builtin_elementwise_fma(w[i], f32x2{-2.f * sc, -2.f * sc}, f32x2{sc, sc}); }
  SCHED();
#pragma unroll
  for (int i = 0; i < 4; ++i) w[i] = h[i] * w[i];
  SCHED();
  return u32x4{pack2(w[0].x, w[0].y), pack2(w[1].x, w[1].y), pack2(w[2].x, w[2].y), pack2(w[3].x, w[3].y)};
}

typedef unsigned long long u64;
typedef __attribute__((address_space(1))) u64 gu64;
__device__ __forceinline__ u64 sy_ld(const gu64* p) { return __hip_atomic_load(p, __ATOMIC_RELAXED, __HIP_MEMORY_SCOPE_AGENT); }
__device__ __forceinline__ void sy_st(gu64* p, u64 v) { __hip_atomic_store(p, v, __ATOMIC_RELAXED, __HIP_MEMORY_SCOPE_AGENT); }

__device__ __forceinline__ void mlp_role(int lane, int wave, int mt, int nb0, const u32x2 (&bias)[7],
                                         unsigned char* __restrict__ ws, gu64* early, unsigned aep) {
  const int c = lane & 15, g = lane >> 4, q4 = (lane >> 2) & 3, p4 = lane & 3;
  const int wm = wave >> 1, wn = wave & 1;
  MlpOps o;
#pragma unroll
  for (int n = 0; n < 7; ++n) o.bias[n] = bias[n];
  o.xa = wm * 4 * 2048 + c * 128 + ((g ^ ((c >> 1) & 7)) * 16);
  o.wa = X_BYTES + ((g >> 1) * 16 + (g & 1) * 4 + q4) * W_ROW + wn * 8 * 32 + 8 * p4;
  o.d6 = wn ? (unsigned)-32 : 192u;
  STAMP(ws, 0, 1);
  k_loop(o);
  STAMP(ws, 0, 2);

  const rsrc_t ro = out_rsrc(ws + A2_OFF, (unsigned)A2_BYTES);
  const unsigned so = ((mt * 2 + wm) * KS2 + 20 + (nb0 >> 1)) * 4096;
  const unsigned vo = lane * 16;
  float sc[4];
#pragma unroll
  for (int r = 0; r < 4; ++r) sc[r] = 1.f - *(const LAS float*)(uintptr_t)(K1_G + (wm * 64 + r * 16 + c) * 4);
#pragma unroll
  for (int j = 0; j < 3; ++j) {
    unsigned vj = vo + so + (wn * 4 + j) * 4096;
    if (j == 2 && wn == 1 && nb0 >= 84) vj = 0x40000000u;
    OPAQUE(vj);
#pragma unroll
    for (int r = 0; r < 4; ++r) {
      const f32x4 h0 = o.a.c[r][2 * j], h1 = o.a.c[r][2 * j + 1];
      bst16<ST_AUX>(ro, vj + r * 1024, mish8(h0, h1, sc[r], sc[r]));
    }
    SCHED();
  }
  if (wn == 1 && nb0 >= 84) WAIT_V(0); else WAIT_V(4);
  if (lane == 0) sy_st(early, *(const LAS u64*)(uintptr_t)aep + 1);
  const unsigned v6 = so + 3 * 4096 + wn * 512 + (g & 1) * 1024 + (g >> 1) * 256 + c * 16;
#pragma unroll
  for (int r = 0; r < 4; r += 2) {
    const f32x4 h0 = o.a.c[r][6], h1 = o.a.c[r + 1][6];
    const u32x4 m = mish8(h0, h1, sc[r], sc[r + 1]);
    bst16<ST_AUX>(ro, v6 + r * 1024, widen_pair(u32x2{m.x, m.y}, u32x2{m.z, m.w}));
  }
}

__device__ __forceinline__ int w2cat_row(int beta, int rho) {
  const int r0 = 20 * beta + rho, r1 = 20 * (4 * (beta - 32) + (rho >> 2)) + 16 + (rho & 3), r2 = 16 * (beta - 40) + rho;
  return beta < 32 ? r0 : (beta < 40 ? r1 : r2);
}

__device__ __forceinline__ int kb_of(int t) { return 20 + 7 * t - (t == 7); }
__device__ __forceinline__ int ks_front(int w, int n) {
  if (n < 3) { const int e = 20 + 3 * w + n; return kb_of(e >> 2) + (e & 1) + 2 * (e & 2); }
  if (n < 7) return kb_of(2 * w + ((n - 3) >> 1)) + 2 + ((n - 3) & 1);
  return 4 + 4 * w + n - 7;
}
__device__ __forceinline__ int ks_back(int lw, int n) {
  if (n < 5) return kb_of(n) + (lw & 1) + 2 * (lw & 2);
  const int q = 4 * (n - 5) + lw;
  return q < 6 ? kb_of(q) + 6 : (q < 10 ? q - 6 : 0x40000);
}
constexpr int P2_REG = 7 * 5120;
static_assert(4 * P2_REG <= EX_OFF, "phase 2 re-uses the phase 1 ring");

typedef u32x4 __attribute__((aligned(8))) u32x4_a8;
constexpr int POLL_SLEEP = 4;
__device__ __forceinline__ bool fb_up(const gu64* sy, int lane) {
  bool ok = true;
#pragma unroll
  for (int i = 0; i < 4; ++i) ok &= sy_ld(sy + SY_FB + 64 * i + lane) == sy_ld(sy + SY_BASE + 64 * i + lane) + 1;
  return __all(ok);
}
#define ACQUIRE() __builtin_amdgcn_fence(__ATOMIC_ACQUIRE, "wavefront")
__device__ __forceinline__ void ld16_dev(u32x4& r, unsigned voff, u32x4 rsrc, unsigned soff) {
  asm volatile("buffer_load_dwordx4 %0, %1, %2, %3 offen sc1" : "=v"(r) : "v"(voff), "s"(rsrc), "s"(soff) : "memory");
}
__device__ __forceinline__ void ld8_dev(u64& r, const gu64* p) { asm volatile("global_load_dwordx2 %0, %1, off sc1" : "=v"(r) : "v"(p) : "memory"); }
__device__ __forceinline__ void ld8_buf(u64& r, unsigned voff, u32x4 rsrc, unsigned soff) {
  asm volatile("buffer_load_dwordx2 %0, %1, %2, %3 offen sc1" : "=v"(r) : "v"(voff), "s"(rsrc), "s"(soff) : "memory");
}
#define PIN(x) asm volatile("" : "+v"(x))
template <int N> __device__ __forceinline__ void wait_v() { asm volatile("s_waitcnt vmcnt(%0)" :: "i"(N) : "memory"); }

__device__ __forceinline__ void mma20(f32x4 (&acc)[4][5], const u32x4 (&a)[4], const u32x4 (&b)[5]) {
#pragma unroll
  for (int i = 0; i < 4; ++i)
#pragma unroll
    for (int j = 0; j < 5; ++j) asm volatile("v_mfma_f32_16x16x32_bf16 %0, %1, %2, %0" : "+v"(acc[i][j]) : "v"(b[j]), "v"(a[i]));
}
__device__ __forceinline__ void acc_zero(f32x4 (&acc)[4][5]) {
#pragma unroll
  for (int i = 0; i < 4; ++i)
#pragma unroll
    for (int j = 0; j < 5; ++j) { acc[i][j] = f32x4{0.f, 0.f, 0.f, 0.f}; OPAQUE(acc[i][j]); }
  asm volatile("s_nop 1");
}
#define ACC_OUT() do { SCHED(); asm volatile("s_nop 7\n\ts_nop 3"); SCHED(); } while (0)
template <int N> __device__ __forceinline__ void wait_pin(u32x4 (&a)[4]) {
  asm volatile("s_waitcnt vmcnt(%4)" : "+v"(a[0]), "+v"(a[1]), "+v"(a[2]), "+v"(a[3]) : "i"(N) : "memory");
}

struct Stream2 {
  u32x4 rs;
  unsigned va[4], vo, sa, sb;
  int lw;
  u32x4 fb[5][5], fa[4][4];
  f32x4 acc[4][5];
  template <int N> __device__ __forceinline__ void load_b() {
    const unsigned so = sb + ks_back(lw, N) * 5120;
#pragma unroll
    for (int j = 0; j < 5; ++j) ld16_dev(fb[N % 5][j], vo, rs, so + j * 1024);
  }
  template <int N> __device__ __forceinline__ void load_a() {
    const unsigned so = sa + ks_back(lw, N) * 4096;
#pragma unroll
    for (int i = 0; i < 4; ++i) ld16_dev(fa[N % 4][i], va[i], rs, so);
  }
  template <int N, int YOUNGER> __device__ __forceinline__ void step() {
    wait_pin<YOUNGER>(fa[N % 4]);
#pragma unroll
    for (int j = 0; j < 5; ++j) PIN(fb[N % 5][j]);
    mma20(acc, fa[N % 4], fb[N % 5]);
    SCHED();
  }
  __device__ __forceinline__ void run() {
    load_a<0>(); load_a<1>(); load_a<2>(); load_a<3>();
    step<0, 12>(); step<1, 8>();
    load_a<4>(); load_b<5>(); lds_wait(0, 2u); ACQUIRE(); load_a<5>(); load_b<6>();
    step<2, 22>(); step<3, 18>();
    load_b<7>(); lds_wait(0, 3u); ACQUIRE(); load_a<6>(); load_a<7>();
    step<4, 27>(); step<5, 18>(); step<6, 4>(); step<7, 0>();
    ACC_OUT();
  }
};

__global__ __launch_bounds__(512, 2) void fused_kernel(const unsigned short* __restrict__ x, const unsigned short* __restrict__ wqkv,
                                                       const unsigned short* __restrict__ wo, const unsigned short* __restrict__ w1,
                                                       const unsigned short* __restrict__ b1, const unsigned short* __restrict__ w2,
                                                       const unsigned short* __restrict__ gate_w, unsigned char* ws,
                                                       unsigned short* __restrict__ out) {
  extern __shared__ __attribute__((aligned(16))) unsigned char smem[];
  const int lane = threadIdx.x & 63;
  const int wave = __builtin_amdgcn_readfirstlane(threadIdx.x >> 6);
  const int b = blockIdx.x, xcd = b & 7, idx = b >> 3;
  const int mt = (xcd & 1) * 8 + (idx & 7);
  const int nsel = idx >> 3;
  const int nt = (xcd >> 1) * 2 + (nsel & 1);
  const bool attn = nsel < 2;
  const int m0 = mt * 128;
  const int nb0 = nt < 7 ? nt * 14 : 96;
  const int L = mt * 16 + (attn ? 0 : 8) + nt;
  const int mt2 = (xcd & 3) * 8 + (idx & 7);
  const int nt2 = (xcd >> 2) * 4 + (idx >> 3);
  gu64* sy = (gu64*)(ws + SY_OFF);
  STAMP(ws, 0, 0);
  if (idx == 16 && wave < 4) {
    uint64_t pc;
    asm volatile("s_getpc_b64 %0" : "=s"(pc));
    const u32x4 rc = make_rsrc((const void*)(pc & ~(uint64_t)255), CODE_BYTES);
    dma_touch(K1_PF, (unsigned)(wave * 64 + lane) * 128u, rc, 0);
    dma_touch(K1_PF, (unsigned)(wave * 64 + lane) * 128u, rc, 32768);
  }

  if (__builtin_expect(wave >= 4, 1)) {
    const int lw = wave - 4;
    u64 pf[4], pb[4];
    {
      Stager st;
      st.rx = make_rsrc(x, S_TOK * D * 2);
      st.vx = (unsigned)((8 * lw + (lane >> 3)) * (D * 2) + (((lane & 7) ^ ((4 * lw + (lane >> 4)) & 7)) * 16));
      st.sx = (unsigned)(m0 * D * 2);
      st.lx = lw * 1024;
      stage_x(st, 0);
      if (lw == 0) ld8_dev(pb[0], sy + SY_BASE + L);
      st.rw = make_rsrc(attn ? wqkv : w1, attn ? D * 3 * D * 2 : D * F * 2);
      const unsigned rowbytes = attn ? 3 * D * 2 : F * 2, secstride = attn ? D * 2 - 160 : 0;
      const int chmax = attn ? 29 : (nt < 6 ? 27 : 23);
      st.wstep = 64u * rowbytes;
      st.sw = attn ? nt * 160 : nb0 * 32;
      st.lw = X_BYTES + lw * 1024;
#pragma unroll
      for (int j = 0; j < 7; ++j) st.vw[j] = w_src(lw + 4 * j, lane, rowbytes, secstride, chmax);
      {
        unsigned v4w = w_src(26 + lw, lane, rowbytes, secstride, chmax);
        const bool hx = lw < 2;
        st.v4 = hx ? st.vx : v4w;
        st.r4 = hx ? st.rx : st.rw;
        st.s4 = hx ? (attn ? (m0 ? st.sx - 16 * D * 2 : 0u) : 0x40000000u) : st.sw;
        st.step4 = hx ? 128u : st.wstep;
        st.l4 = hx ? (16 + lw) * 1024 : X_BYTES + (26 + lw) * 1024;
      }
      stage_w(st, 0); stage_next(st);
      stage_x(st, STAGE); stage_w(st, STAGE); stage_next(st);
      constexpr int T0 = 0;
      const int unit = lw < 2 ? lw * 256 + b : 384 + b;
      const bool tile = lw < 2 || (lw == 2 && b >= 128 && b < 208);
      const int uks = unit >> 3;
      const unsigned lt = EX_OFF + lw * 5120;
      unsigned vt[5];
#pragma unroll
      for (int i = 0; i < 5; ++i) {
        unsigned ci = i * 64 + lane, row = (ci * 205u) >> 11, ch = ci - row * 10u;
        vt[i] = (unsigned)w2cat_row(2 * uks + (row >> 4), row & 15) * (D * 2) + ch * 16;
        OPAQUE(vt[i]);
      }
      WAIT_V(12);
      STAMP(ws, 0, 1);
      BAR();
      stage_x(st, 2 * STAGE); stage_w(st, 2 * STAGE); stage_next(st);
      unsigned sb = 0;
#pragma unroll 1
      for (int t = 0; t < NT1 - 3; ++t) {
        if (tile && (t == T0 + 1 || t == T0 + 3)) WAIT_V(17); else WAIT_V(12);
        BAR();
        if (t == T0 + 4 && lw == 0) { PIN(pb[0]); if (lane == 0) sy_st(sy + SY_FB + L, pb[0] + 1); }
        stage_x(st, sb); stage_w(st, sb); stage_next(st);
        if (t == T0 && tile) {
          const u32x4 r2 = make_rsrc(uks < 20 ? wo : w2, uks < 20 ? D * D * 2 : F * D * 2);
#pragma unroll
          for (int i = 0; i < 5; ++i) dma16(lt + i * 1024, vt[i], r2, (unit & 7) * 160);
        }
        if (t == T0 + 2 && tile) {
          const bool half = uks < 20 || uks == 71 || (uks < 66 && (uks - 23) % 7 == 0);
          const unsigned ra = lt + ((half ? 8 : 4) * (lane >> 4) + ((lane >> 2) & 3)) * 160 + (lane & 3) * 40;
          const unsigned rb = ra + (half ? 4 : 16) * 160;
          unsigned char* bp = ws + B2_OFF + ((size_t)(unit & 7) * KS2 + uks) * 5120 + lane * 16;
#pragma unroll
          for (int i = 0; i < 5; ++i) st16(bp + i * 1024, cat(lds_tr(ra + 8 * i), lds_tr(rb + 8 * i)));
        }
        sb += STAGE; if (sb == NSTAGE * STAGE) sb = 0;
      }
    }
    Stream2 ld;
    ld.rs = make_rsrc(ws, (unsigned)(A2_BYTES + B2_BYTES + 8192));
    ld.vo = lane * 16;
#pragma unroll
    for (int i = 0; i < 4; ++i) ld.va[i] = lane * 16 + ((i + lw) & 3) * 1024;
    const unsigned sb0 = (unsigned)B2_OFF + nt2 * (KS2 * 5120);
    ld.sa = (unsigned)A2_OFF + mt2 * (KS2 * 4096); ld.sb = sb0; ld.lw = lw;
    WAIT_V(12); BAR();
    if (lw == 0) {
#pragma unroll
      for (int i = 0; i < 4; ++i) { ld8_dev(pf[i], sy + SY_FB + 64 * i + lane); ld8_dev(pb[i], sy + SY_BASE + 64 * i + lane); }
    }
    WAIT_V(0);
    if (lw == 0) {
      bool ok = true;
#pragma unroll
      for (int i = 0; i < 4; ++i) {
        PIN(pf[i]); PIN(pb[i]); ok &= pf[i] == pb[i] + 1;
        *(LAS u64*)(uintptr_t)(K1_BASES + i * 512 + lane * 8) = pb[i];
      }
      if (!__all(ok)) {
#pragma unroll 1
        for (int i = 0; i < SPIN_CAP; ++i) { if (fb_up(sy, lane)) break; __builtin_amdgcn_s_sleep(POLL_SLEEP); }
      }
      WAIT_L0();
    }
    BAR();
    ACQUIRE();
    if (lw == 1 || lw == 2)
      asm volatile("s_mov_b32 m0, %0\n\ts_nop 0\n\tbuffer_load_dword %1, %2, %3 offen sc1 lds"
                   :: "s"(K1_G + (lw - 1) * 256), "v"(lane * 4), "s"(ld.rs), "s"((unsigned)G_OFF + (m0 + (lw - 1) * 64) * 4) : "memory");
    ld.load_b<0>(); ld.load_b<1>(); ld.load_b<2>();
    WAIT_V(15); BAR();
    ld.load_b<3>(); ld.load_b<4>();
    STAMP(ws, 0, 2);
    auto stage_b = [&](int n) {
      const int ks = ks_front(lw, n);
#pragma unroll
      for (int j = 0; j < 5; ++j) dma16_dev(lw * P2_REG + n * 5120 + j * 1024, ld.vo, ld.rs, sb0 + ks * 5120 + j * 1024);
    };
    constexpr int THIN = 5;
    stage_b(0); stage_b(1);
    wait_v<THIN>(); stage_b(2); wait_v<THIN>(); stage_b(3); wait_v<THIN>(); stage_b(4); wait_v<THIN>();
    const int Lp = (mt2 >> 1) * 16 + 8 + ((lane >> 1) & 7);
    const gu64* fm = sy + SY_FE + Lp * 4 + (mt2 & 1) * 2 + (lane & 1);
    stage_b(5); wait_v<THIN>(); stage_b(6);
    if (lw == 0) { ld8_dev(pf[0], fm); ld8_dev(pb[0], sy + SY_BASE + Lp); }
    acc_zero(ld.acc);
    WAIT_V(0);
    lds_post(4 + lw, 1u, lane);
    STAMP(ws, 0, 3);
    if (lw == 0) {
      PIN(pf[0]); PIN(pb[0]);
#pragma unroll 1
      for (int k = 0; k < SPIN_CAP && !__all(pf[0] == pb[0] + 1); ++k) { __builtin_amdgcn_s_sleep(POLL_SLEEP); pf[0] = sy_ld(fm); }
      lds_post(0, 1u, lane);
    } else lds_wait(0, 1u);
    ACQUIRE();
    STAMP(ws, 0, 4);
    ld.run();
    STAMP(ws, 0, 5);
    lds_wait(8 + lw, 1u);
    unsigned rr = lw * P2_REG + lane * 16;
    OPAQUE(rr);
#pragma unroll
    for (int i = 0; i < 4; ++i)
#pragma unroll
      for (int j = 0; j < 5; ++j)
        if (i > 0 || j < 2) *(LAS f32x4*)(uintptr_t)(rr + (i == 0 ? 18 + j : 15 + i * 5 + j) * 1024) = ld.acc[i][j];
    WAIT_L0();
    BAR();
    f32x4 r[3];
#pragma unroll
    for (int j = 2; j < 5; ++j) r[j - 2] = ld.acc[0][j] + *(const LAS f32x4*)(uintptr_t)(rr + (j - 2) * 1024);
#pragma unroll
    for (int d = 1; d < 4; ++d) {
      unsigned rq = ((lw - d) & 3) * P2_REG + lane * 16;
      OPAQUE(rq);
#pragma unroll
      for (int j = 2; j < 5; ++j)
        r[j - 2] += *(const LAS f32x4*)(uintptr_t)(rq + (d * 5 + j - 2) * 1024) + *(const LAS f32x4*)(uintptr_t)(rq + (15 + d * 5 + j) * 1024);
    }
    unsigned* op = (unsigned*)(out + (size_t)(mt2 * 64 + lw * 16 + (lane & 15)) * D + nt2 * 80 + (lane >> 4) * 20);
    *(u32x4_a8*)(op + 4) = u32x4{pack2(r[0][0], r[0][1]), pack2(r[0][2], r[0][3]), pack2(r[1][0], r[1][1]), pack2(r[1][2], r[1][3])};
    *(u32x2*)(op + 8) = u32x2{pack2(r[2][0], r[2][1]), pack2(r[2][2], r[2][3])};
    STAMP(ws, 0, 6);
    return;
  }

  const unsigned aep = K1_BASES + L * 8;
  if (threadIdx.x < 16) *(LAS unsigned*)(uintptr_t)(K1_NEWS + threadIdx.x * 4) = 0u;
  u32x2 bias[7];
  if (!attn) {
    const u32x2* bp = (const u32x2*)(b1 + (lane >> 4) * 4);
#pragma unroll
    for (int n = 0; n < 7; ++n) bias[n] = bp[min(nb0 + (n < 6 ? (wave & 1) * 8 + n : 6 + (wave & 1)), 107) * 4];
  }
  if (attn) {
    const int tok = m0 + nt * 16 + wave * 4 + (lane >> 4);
    const u32x4* gp = (const u32x4*)gate_w + (lane & 15);
    const u32x4* xr = (const u32x4*)(x + (size_t)tok * D) + (lane & 15);
    float sg = 0.f;
#pragma unroll
    for (int j = 0; j < 5; ++j) {
      const u32x4 a = xr[16 * j], w = gp[16 * j];
#pragma unroll
      for (int e = 0; e < 4; ++e) { sg = fmaf(bf_lo(a[e]), bf_lo(w[e]), sg); sg = fmaf(bf_hi(a[e]), bf_hi(w[e]), sg); }
    }
#pragma unroll
    for (int o = 8; o >= 1; o >>= 1) sg += __shfl_xor(sg, o, 64);
    if ((lane & 15) == 0)
      __hip_atomic_store((__attribute__((address_space(1))) float*)(ws + G_OFF) + tok, 1.f / (1.f + __expf(-sg)),
                         __ATOMIC_RELAXED, __HIP_MEMORY_SCOPE_AGENT);
  }
  if (!attn) {
#pragma unroll
    for (int n = 0; n < 7; ++n) PIN(bias[n]);
    const int wg = b & 127;
    const int pw = wg * 4 + wave;
    const int li = pw * 28 + (lane < 28 ? lane : 0);
    dma_touch(K1_PF, (unsigned)((li / 7) * (D * 2) + (3 + li % 7) * 128), make_rsrc(x, S_TOK * D * 2), 0);
    {
      const int q = ((idx & 15) * 4 + wave) * 63 + (lane < 63 ? lane : 0), row = q / 9, sl = q - row * 9;
      dma_touch(K1_PF, (unsigned)(row * (3 * D * 2) + (sl / 3) * (D * 2) + (sl % 3) * 128 + (xcd >> 1) * 320), make_rsrc(wqkv, D * 3 * D * 2), 192 * 3 * D * 2);
    }
    dma_touch(K1_PF, (unsigned)((pw * 24 + (lane < 24 ? lane : 0)) * 128), make_rsrc(w1, D * F * 2), 192 * F * 2);
  }
  WAIT_L0();
  if (attn) {
    AttnPacked pk;
    if (wave == 0) attn_main<0>(lane, pk, ws);
    else if (wave == 1) attn_main<1>(lane, pk, ws);
    else if (wave == 2) attn_main<2>(lane, pk, ws);
    else attn_main<3>(lane, pk, ws);
    attn_epilogue(lane, wave, mt, nt, pk, ws);
  } else {
    mlp_role(lane, wave, mt, nb0, bias, ws, sy + SY_FE + L * 4 + wave, aep);
  }
  const unsigned ep_lo = __builtin_amdgcn_readfirstlane(*(const LAS unsigned*)(uintptr_t)aep);
  const unsigned ep_hi = __builtin_amdgcn_readfirstlane(*(const LAS unsigned*)(uintptr_t)(aep + 4));
  const u64 epoch = (((u64)ep_hi << 32) | ep_lo) + 1;
  WAIT_V(0);
  if (lane == 0) sy_st(sy + SY_FA + L * 4 + wave, epoch);
  STAMP(ws, 0, 3);

  int ln = lane;
  OPAQUE(ln);
  const u32x4 rs = make_rsrc(ws, (unsigned)SY_OFF + 32768);
  const unsigned sa = (unsigned)A2_OFF + mt2 * (KS2 * 4096);
  unsigned va[4];
#pragma unroll
  for (int i = 0; i < 4; ++i) va[i] = ln * 16 + ((i + wave) & 3) * 1024;
  u32x4 fa[4][4], eb[4][5];
  const int La = (mt2 >> 1) * 16 + (ln < 32 ? (ln >> 2) : 8 + ((ln >> 1) & 7));
  const gu64* fat = sy + SY_FA + La * 4 + (ln < 32 ? (ln & 3) : (mt2 & 1) * 2 + (ln & 1));
  u64 pp[4];
  u64 up = 0;
  const unsigned sbb = (unsigned)B2_OFF + nt2 * (KS2 * 5120), vo = ln * 16;
#define P2_B(n) { _Pragma("unroll") for (int j = 0; j < 5; ++j) ld16_dev(eb[n - 7][j], vo, rs, sbb + ks_front(wave, n) * 5120 + j * 1024); }
  P2_B(7) P2_B(8) P2_B(9)
  f32x4 acc[4][5];
  acc_zero(acc);
  unsigned rb = wave * P2_REG + ln * 16;
  OPAQUE(rb);
#define P2_A(n) { _Pragma("unroll") for (int i = 0; i < 4; ++i) ld16_dev(fa[(n) & 3][i], va[i], rs, sa + ks_front(wave, n) * 4096); }
#define P2_STEP(n, CNT) { u32x4 fb[5]; _Pragma("unroll") for (int j = 0; j < 5; ++j) fb[j] = lds_read128(rb + n * 5120 + j * 1024); \
    wait_pin<CNT>(fa[(n) & 3]); mma20(acc, fa[(n) & 3], fb); SCHED(); }
#define P2_EXTRA(n, CNT) { wait_pin<CNT>(fa[(n) & 3]); _Pragma("unroll") for (int j = 0; j < 5; ++j) PIN(eb[n - 7][j]); \
    mma20(acc, fa[(n) & 3], eb[n - 7]); SCHED(); }
  lds_wait(4 + wave, 1u);
  lds_wait(0, 1u);
  ACQUIRE();
  if (wave == 0) { ld8_dev(pp[0], fat); ld8_dev(pp[1], sy + SY_BASE + La); }
  P2_A(0) P2_A(1) P2_A(2)
  if (wave == 0) {
    WAIT_V(12);
    PIN(pp[0]); PIN(pp[1]);
    up = __ballot(pp[0] == pp[1] + 1);
#pragma unroll 1
    for (int k = 0; k < SPIN_CAP && (up >> 32) != 0xffffffffull; ++k) { __builtin_amdgcn_s_sleep(POLL_SLEEP); up = __ballot(sy_ld(fat) == pp[1] + 1); }
    lds_post(0, up == ~0ull ? 3u : 2u, ln);
  }
  STAMP(ws, 0, 4);
  P2_STEP(0, 8)
  lds_wait(0, 2u);
  ACQUIRE();
  if (wave == 0) ld8_dev(pp[0], fat);
  P2_A(3) P2_A(4)
  P2_STEP(1, 12) P2_A(5)
  P2_STEP(2, 12)
  if (wave == 0) {
    PIN(pp[0]);
    up |= __ballot(pp[0] == pp[1] + 1);
#pragma unroll 1
    for (int k = 0; k < SPIN_CAP && up != ~0ull; ++k) { up = __ballot(sy_ld(fat) == pp[1] + 1); if (up != ~0ull) __builtin_amdgcn_s_sleep(POLL_SLEEP); }
    lds_post(0, 3u, ln);
    if (ln == 0) sy_st(sy + SY_PASS + L, epoch);
  }
  P2_A(6)
  P2_STEP(3, 12)
  lds_wait(0, 3u);
  ACQUIRE();
  P2_A(7)
  P2_STEP(4, 12) P2_A(8)
  P2_STEP(5, 12) P2_A(9)
  P2_STEP(6, 12) P2_B(10) P2_A(10)
  P2_EXTRA(7, 17) P2_EXTRA(8, 13) P2_EXTRA(9, 9) P2_EXTRA(10, 0)
  ACC_OUT();
#undef P2_A
#undef P2_B
#undef P2_STEP
#undef P2_EXTRA
  if (wave == 0) {
#pragma unroll
    for (int i = 0; i < 4; ++i) ld8_buf(pp[i], ln * 8, rs, (unsigned)SY_OFF + (SY_PASS + 64 * i) * 8);
  }
#pragma unroll
  for (int i = 0; i < 4; ++i)
#pragma unroll
    for (int j = 0; j < 5; ++j)
      if (i > 0 || j >= 2) *(LAS f32x4*)(uintptr_t)(rb + (i * 5 + j - 2) * 1024) = acc[i][j];
  WAIT_L0();
  lds_post(8 + wave, 1u, ln);
  STAMP(ws, 0, 5);
  BAR();
  {
    f32x4 r[2];
#pragma unroll
    for (int j = 0; j < 2; ++j) r[j] = acc[0][j] + *(const LAS f32x4*)(uintptr_t)(rb + (18 + j) * 1024);
#pragma unroll
    for (int d = 1; d < 4; ++d) {
      unsigned rq = ((wave - d) & 3) * P2_REG + ln * 16;
      OPAQUE(rq);
#pragma unroll
      for (int j = 0; j < 2; ++j)
        r[j] += *(const LAS f32x4*)(uintptr_t)(rq + (d * 5 + j - 2) * 1024) + *(const LAS f32x4*)(uintptr_t)(rq + (15 + d * 5 + j) * 1024);
    }
    if (wave == 0) WAIT_V(0);
    unsigned* op = (unsigned*)(out + (size_t)(mt2 * 64 + wave * 16 + (ln & 15)) * D + nt2 * 80 + (ln >> 4) * 20);
    *(u32x4_a8*)op = u32x4{pack2(r[0][0], r[0][1]), pack2(r[0][2], r[0][3]), pack2(r[1][0], r[1][1]), pack2(r[1][2], r[1][3])};
  }
  if (wave == 0) {
    u64 wb[4];
#pragma unroll
    for (int i = 0; i < 4; ++i) { PIN(pp[i]); wb[i] = *(const LAS u64*)(uintptr_t)(K1_BASES + i * 512 + ln * 8); }
#pragma unroll 1
    for (int k = 0; k < SPIN_CAP; ++k) {
      if (__all(pp[0] == wb[0] + 1 && pp[1] == wb[1] + 1 && pp[2] == wb[2] + 1 && pp[3] == wb[3] + 1)) break;
      __builtin_amdgcn_s_sleep(POLL_SLEEP);
#pragma unroll
      for (int i = 0; i < 4; ++i) pp[i] = sy_ld(sy + SY_PASS + 64 * i + ln);
    }
    if (ln == 0) sy_st(sy + SY_BASE + L, epoch);
  }
  STAMP(ws, 0, 6);
}

}

extern "C" void kernel_launch(void* const* d_in, const int* in_sizes, int n_in, void* d_out, int out_size, void* d_ws,
                              size_t ws_size, hipStream_t stream) {
  const unsigned short* x = (const unsigned short*)d_in[0];
  const unsigned short* wqkv = (const unsigned short*)d_in[1];
  const unsigned short* wo = (const unsigned short*)d_in[2];
  const unsigned short* w1 = (const unsigned short*)d_in[4];
  const unsigned short* b1 = (const unsigned short*)d_in[5];
  const unsigned short* w2 = (const unsigned short*)d_in[6];
  const unsigned short* gate_w = (const unsigned short*)d_in[7];
  (void)hipFuncSetAttribute((const void*)fused_kernel, hipFuncAttributeMaxDynamicSharedMemorySize, K1_LDS);
  fused_kernel<<<256, 512, K1_LDS, stream>>>(x, wqkv, wo, w1, b1, w2, gate_w, (unsigned char*)d_ws, (unsigned short*)d_out);
}
```
